```python
import torch

def _softmax(x, axis=-1):
    return torch.softmax(x, dim=axis)
def _gelu(x):
    return torch.nn.functional.gelu(x, approximate="tanh")
def _silu(x):
    return torch.nn.functional.silu(x)
def _sigmoid(x):
    return torch.sigmoid(x)
def _layernorm(x, axis=-1, eps=1e-5):
    m = x.mean(dim=axis, keepdim=True)
    v = x.var(dim=axis, keepdim=True, unbiased=False)
    return (x - m) / torch.sqrt(v + eps)
def _scatter_add(tgt, idx, src, axis=0):
    return tgt.clone().index_add_(axis, idx, src)
def _segment_sum(src, seg, n):
    out = torch.zeros((n,) + src.shape[1:], dtype=src.dtype)
    return out.index_add_(0, seg, src)
def _pad(x, pw):
    flat = tuple(v for pair in reversed(pw) for v in pair)
    return torch.nn.functional.pad(x, flat)

kernel_name = 'standalone__sequential_behavior_din__recsys'

def setup_inputs():

    _w = (lambda a: (lambda t: t.to(torch.bfloat16) if t.is_floating_point() else t)(torch.as_tensor(a)))
    B, L, D = 4096, 50, 64
    H = D
    return {
        "target": _w((lambda *s: torch.randn(*s, dtype=torch.bfloat16) * 3.2)(B, D)),
        "hist": _w((lambda *s: torch.randn(*s, dtype=torch.bfloat16) * 3.2)(B, L, D)),
        "mask": _w((lambda lo, hi, *s: torch.randint(lo, hi, s))(0, 2, B, L).to(float)),
        "w1": _w((lambda *s: torch.randn(*s, dtype=torch.bfloat16) * 3.2)(4 * D, H) * (4 * D) ** -0.5),
        "b1": _w((lambda *s: torch.randn(*s, dtype=torch.bfloat16) * 3.2)(H) * 0.1),
        "w2": _w((lambda *s: torch.randn(*s, dtype=torch.bfloat16) * 0.05)(H, 1) * H ** -0.5),
        "b2": _w((lambda *s: torch.randn(*s, dtype=torch.bfloat16) * 0.05)(1) * 0.1),
    }

def reference(target, hist, mask, w1, b1, w2, b2):
    L = hist.shape[1]
    tgt = (lambda x, n, axis: torch.repeat_interleave(x, n, dim=axis))(target[:, None, :], L, axis=1)
    feat = torch.concatenate([hist, tgt, hist - tgt, hist * tgt], axis=-1)
    h = _sigmoid(feat @ w1 + b1)
    scores = (h @ w2 + b2)[..., 0] * mask
    return torch.einsum('bl,bld->bd', scores, hist)
```

```cpp
#include <hip/hip_runtime.h>
#include <hip/hip_bf16.h>

static __device__ __forceinline__ float bf2f(unsigned short u) { return __uint_as_float(((unsigned)u) << 16); }
static __device__ __forceinline__ unsigned short f2bf(float f) {
    unsigned u = __float_as_uint(f);
    u += 0x7fffu + ((u >> 16) & 1u);
    return (unsigned short)(u >> 16);
}

__global__ __launch_bounds__(64) void din_naive(const unsigned short* __restrict__ tgt, const unsigned short* __restrict__ hist,
                          const unsigned short* __restrict__ mask, const unsigned short* __restrict__ w1,
                          const unsigned short* __restrict__ b1, const unsigned short* __restrict__ w2,
                          const unsigned short* __restrict__ b2, unsigned short* __restrict__ out, int L) {
    __shared__ float sh[64];
    __shared__ float st[64];
    __shared__ float red[64];
    int b = blockIdx.x, n = threadIdx.x;
    st[n] = bf2f(tgt[b * 64 + n]);
    float acc = 0.f;
    float bb1 = bf2f(b1[n]), ww2 = bf2f(w2[n]), bb2 = bf2f(b2[0]);
    for (int l = 0; l < L; ++l) {
        __syncthreads();
        sh[n] = bf2f(hist[((size_t)b * L + l) * 64 + n]);
        __syncthreads();
        float z = 0.f;
        for (int k = 0; k < 64; ++k) {
            float h = sh[k], t = st[k];
            z += h * bf2f(w1[k * 64 + n]);
            z += t * bf2f(w1[(64 + k) * 64 + n]);
            z += (h - t) * bf2f(w1[(128 + k) * 64 + n]);
            z += (h * t) * bf2f(w1[(192 + k) * 64 + n]);
        }
        z += bb1;
        float s = 1.f / (1.f + __expf(-z));
        float p = s * ww2;
        for (int o = 32; o > 0; o >>= 1) p += __shfl_xor(p, o, 64);
        float sc = (p + bb2) * bf2f(mask[b * L + l]);
        acc += sc * sh[n];
    }
    out[b * 64 + n] = f2bf(acc);
}

extern "C" void kernel_launch(void* const* d_in, const int* in_sizes, int n_in,
                              void* d_out, int out_size, void* d_ws, size_t ws_size,
                              hipStream_t stream) {
    const unsigned short* tgt = (const unsigned short*)d_in[0];
    const unsigned short* hist = (const unsigned short*)d_in[1];
    const unsigned short* mask = (const unsigned short*)d_in[2];
    const unsigned short* w1 = (const unsigned short*)d_in[3];
    const unsigned short* b1 = (const unsigned short*)d_in[4];
    const unsigned short* w2 = (const unsigned short*)d_in[5];
    const unsigned short* b2 = (const unsigned short*)d_in[6];
    int B = in_sizes[0] / 64;
    int L = in_sizes[2] / B;
    din_naive<<<B, 64, 0, stream>>>(tgt, hist, mask, w1, b1, w2, b2, (unsigned short*)d_out, L);
}
```

```cpp
#include <hip/hip_runtime.h>

typedef __bf16 bf16x8 __attribute__((ext_vector_type(8)));
typedef __bf16 bf16x2 __attribute__((ext_vector_type(2)));
typedef short s16x4 __attribute__((ext_vector_type(4)));
typedef float f32x4 __attribute__((ext_vector_type(4)));
typedef float f32x2 __attribute__((ext_vector_type(2)));
typedef unsigned u32x4 __attribute__((ext_vector_type(4)));
typedef unsigned u32x2 __attribute__((ext_vector_type(2)));
#define LDS_AS __attribute__((address_space(3)))
#define DI static __device__ __forceinline__

constexpr int LH = 50;
constexpr int NW = 16;
constexpr int HREG = 8192;
constexpr int WBLK = 544;
constexpr int WIMG = 16 * WBLK;
constexpr int WD_OFF = NW * HREG;
constexpr int WAC_OFF = WD_OFF + WIMG;
constexpr int WBC_OFF = WAC_OFF + WIMG;
constexpr int M_OFF = WBC_OFF + WIMG;
constexpr int LDS_TOTAL = M_OFF + NW * 256;

DI unsigned pk(float a, float b) {
    f32x2 p = {a, b};
    return __builtin_bit_cast(unsigned, __builtin_convertvector(p, bf16x2));
}
DI f32x4 mfma(u32x4 a, u32x4 b, f32x4 c) {
    return __builtin_amdgcn_mfma_f32_16x16x32_bf16(__builtin_bit_cast(bf16x8, a), __builtin_bit_cast(bf16x8, b), c, 0, 0, 0);
}
DI u32x2 trread(LDS_AS char* p) {
    return __builtin_bit_cast(u32x2, __builtin_amdgcn_ds_read_tr16_b64_v4i16((LDS_AS s16x4*)p));
}
DI float lo16(unsigned u) { return __uint_as_float(u << 16); }
DI float hi16(unsigned u) { return __uint_as_float(u & 0xffff0000u); }

#define STAMP(i)
#define RSTAMP(i)

#define HNT ""

__global__ __launch_bounds__(1024) void din_kernel(const char* __restrict__ tgt, const char* __restrict__ hist,
                                                   const char* __restrict__ mask, const char* __restrict__ w1,
                                                   const char* __restrict__ b1, const char* __restrict__ w2,
                                                   const char* __restrict__ b2, char* __restrict__ out
) {
    __shared__ __attribute__((aligned(16))) char lds_[LDS_TOTAL];
    LDS_AS char* lds = (LDS_AS char*)lds_;
    const unsigned tid = threadIdx.x, lane = tid & 63;
    const unsigned wave = __builtin_amdgcn_readfirstlane(tid >> 6);
    const unsigned b0 = blockIdx.x * NW;
    const unsigned b = b0 + wave;
    const unsigned c = lane & 15, g = lane >> 4;

    unsigned mv;
    {
        unsigned off = b * (LH * 2) + (lane < LH ? lane : LH - 1) * 2;
        asm volatile("global_load_ushort %0, %1, %2" : "=v"(mv) : "v"(off), "s"(mask));
    }
    u32x2 wa, wb, wc, wd;
    {
        unsigned off = tid * 8;
        unsigned off2 = off + 16384;
        asm volatile("global_load_dwordx2 %0, %4, %6\n\t"
                     "global_load_dwordx2 %2, %5, %6\n\t"
                     "global_load_dwordx2 %1, %4, %7\n\t"
                     "global_load_dwordx2 %3, %5, %7"
                     : "=&v"(wa), "=&v"(wb), "=&v"(wc), "=&v"(wd)
                     : "v"(off), "v"(off2), "s"(w1), "s"(w1 + 8192));
    }
    u32x4 T0, T1, W20, W21;
    unsigned b2v;
    {
        unsigned off = g * 16;
        unsigned offt = off + b * 128;
        unsigned z = 0;
        asm volatile("global_load_dwordx4 %0, %5, %8\n\t"
                     "global_load_dwordx4 %1, %5, %8 offset:64\n\t"
                     "global_load_dwordx4 %2, %6, %9\n\t"
                     "global_load_dwordx4 %3, %6, %9 offset:64\n\t"
                     "global_load_ushort %4, %7, %10"
                     : "=&v"(T0), "=&v"(T1), "=&v"(W20), "=&v"(W21), "=&v"(b2v)
                     : "v"(offt), "v"(off), "v"(z), "s"(tgt), "s"(w2), "s"(b2));
    }
    u32x4 X0, X1;
    u32x2 b1v;
    if (wave < 4) {
        unsigned offx = (b0 + c) * 128 + g * 16;
        unsigned offb = (wave >> 1) * 64 + g * 16 + (wave & 1) * 8;
        asm volatile("global_load_dwordx4 %0, %3, %5\n\t"
                     "global_load_dwordx4 %1, %3, %5 offset:64\n\t"
                     "global_load_dwordx2 %2, %4, %6\n\t"
                     "s_waitcnt vmcnt(12)"
                     : "=&v"(X0), "=&v"(X1), "=&v"(b1v)
                     : "v"(offx), "v"(offb), "s"(tgt), "s"(b1));
    } else {
        asm volatile("s_waitcnt vmcnt(9)" : "=v"(X0), "=v"(X1), "=v"(b1v));
    }
    asm volatile("" : "+v"(mv));
    STAMP(2);

    const bool set = (lane < LH) && ((mv & 0x7fffu) != 0);
    const unsigned long long M = __ballot(set);
    const unsigned rank = __builtin_amdgcn_mbcnt_hi((unsigned)(M >> 32), __builtin_amdgcn_mbcnt_lo((unsigned)M, 0));
    const unsigned cnt = __builtin_popcountll(M);
    const unsigned dest = set ? rank : cnt + lane - rank;
    LDS_AS char* hreg = lds + wave * HREG;
    LDS_AS char* marr = lds + M_OFF + wave * 256;
    *(LDS_AS unsigned*)(hreg + 62 * 128 + ((dest & 7) * 8 + (dest >> 3)) * 4) = set ? lane * 128 : 0x40000000u;
    *(LDS_AS unsigned*)(marr + dest * 4) = set ? mv << 16 : 0u;
    asm volatile("" ::: "memory");
    u32x4 ia = *(LDS_AS u32x4*)(hreg + 62 * 128 + (lane >> 3) * 32);
    u32x4 ib = *(LDS_AS u32x4*)(hreg + 62 * 128 + (lane >> 3) * 32 + 16);
    const unsigned choff = ((lane & 7) ^ ((lane >> 3) & 6)) * 16;
    u32x4 srd;
    {
        unsigned long long hb = (unsigned long long)hist + (unsigned long long)b * (LH * 128);
        srd[0] = (unsigned)hb;
        srd[1] = (unsigned)(hb >> 32) & 0xffffu;
        srd[2] = LH * 128;
        srd[3] = 0x00020000u;
    }
    const unsigned hbase = (unsigned)(size_t)hreg;
    ia += choff;
    asm volatile("s_mov_b32 m0, %0\n\t"
                 "s_nop 0\n\t"
                 "buffer_load_dwordx4 %1, %5, 0 offen lds" HNT "\n\t"
                 "s_add_u32 m0, m0, 0x400\n\t"
                 "s_nop 0\n\t"
                 "buffer_load_dwordx4 %2, %5, 0 offen lds" HNT "\n\t"
                 "s_add_u32 m0, m0, 0x400\n\t"
                 "s_nop 0\n\t"
                 "buffer_load_dwordx4 %3, %5, 0 offen lds" HNT "\n\t"
                 "s_add_u32 m0, m0, 0x400\n\t"
                 "s_nop 0\n\t"
                 "buffer_load_dwordx4 %4, %5, 0 offen lds" HNT
                 :: "s"(hbase), "v"(ia[0]), "v"(ia[1]), "v"(ia[2]), "v"(ia[3]), "s"(srd) : "memory", "scc");
    if (cnt > 32) {
        ib += choff;
        asm volatile("s_add_u32 m0, %0, 0x1000\n\t"
                     "s_nop 0\n\t"
                     "buffer_load_dwordx4 %1, %5, 0 offen lds" HNT "\n\t"
                     "s_add_u32 m0, m0, 0x400\n\t"
                     "s_nop 0\n\t"
                     "buffer_load_dwordx4 %2, %5, 0 offen lds" HNT "\n\t"
                     "s_add_u32 m0, m0, 0x400\n\t"
                     "s_nop 0\n\t"
                     "buffer_load_dwordx4 %3, %5, 0 offen lds" HNT "\n\t"
                     "s_add_u32 m0, m0, 0x400\n\t"
                     "s_nop 0\n\t"
                     "buffer_load_dwordx4 %4, %5, 0 offen lds" HNT "\n\t"
                     "s_waitcnt vmcnt(8)"
                     :: "s"(hbase), "v"(ib[0]), "v"(ib[1]), "v"(ib[2]), "v"(ib[3]), "s"(srd) : "memory", "scc");
    } else {
        asm volatile("s_waitcnt vmcnt(4)" ::: "memory");
    }
    asm volatile("" : "+v"(wa), "+v"(wb), "+v"(wc), "+v"(wd), "+v"(T0), "+v"(T1), "+v"(W20), "+v"(W21), "+v"(b2v),
                      "+v"(X0), "+v"(X1), "+v"(b1v));
    STAMP(3);

    const float KS = -1.4426950408889634f;
    {
        float a[4] = {lo16(wa[0]), hi16(wa[0]), lo16(wa[1]), hi16(wa[1])};
        float bb[4] = {lo16(wb[0]), hi16(wb[0]), lo16(wb[1]), hi16(wb[1])};
        float cc[4] = {lo16(wc[0]), hi16(wc[0]), lo16(wc[1]), hi16(wc[1])};
        float dd[4] = {lo16(wd[0]), hi16(wd[0]), lo16(wd[1]), hi16(wd[1])};
        float ac[4], bc[4], dk[4];
#pragma unroll
        for (int i = 0; i < 4; ++i) {
            float ck = cc[i] * KS;
            ac[i] = __builtin_fmaf(a[i], KS, ck);
            bc[i] = __builtin_fmaf(bb[i], KS, -ck);
            dk[i] = dd[i] * KS;
        }
        const unsigned d = tid >> 4;
        const unsigned dt = 2 * (d >> 5) + ((d >> 2) & 1), gd = (d >> 3) & 3, q = d & 3;
        const unsigned nt = 2 * ((tid >> 3) & 1) + (tid & 1), p = (tid >> 1) & 3;
        LDS_AS char* wp = lds + WD_OFF + (dt * 4 + nt) * WBLK + gd * 128 + q * 32 + p * 8;
        *(LDS_AS u32x2*)(wp) = u32x2{pk(dk[0], dk[1]), pk(dk[2], dk[3])};
        *(LDS_AS u32x2*)(wp + WIMG) = u32x2{pk(ac[0], ac[1]), pk(ac[2], ac[3])};
        *(LDS_AS u32x2*)(wp + 2 * WIMG) = u32x2{pk(bc[0], bc[1]), pk(bc[2], bc[3])};
    }
    asm volatile("s_waitcnt lgkmcnt(0)" ::: "memory");
    __builtin_amdgcn_s_barrier();
    asm volatile("" ::: "memory");
    STAMP(4);

    LDS_AS char* wl = lds + WD_OFF + lane * 8;
    if (wave < 4) {
        LDS_AS char* wq = wl + 2 * WIMG + wave * WBLK;
        u32x2 f00 = trread(wq), f01 = trread(wq + 4 * WBLK), f10 = trread(wq + 8 * WBLK), f11 = trread(wq + 12 * WBLK);
        f32x4 acc = {lo16(b1v[0]) * KS, hi16(b1v[0]) * KS, lo16(b1v[1]) * KS, hi16(b1v[1]) * KS};
        acc = mfma(u32x4{f00[0], f00[1], f01[0], f01[1]}, X0, acc);
        acc = mfma(u32x4{f10[0], f10[1], f11[0], f11[1]}, X1, acc);
        *(LDS_AS f32x4*)(lds + WBC_OFF + ((c >> 3) * 4 + wave) * WBLK + ((c & 7) * 4 + g) * 16) = acc;
    }

    u32x4 WA[4][2];
    {
        const bool sp = (c >> 2) == g;
        const unsigned i = c & 3;
        const unsigned half = (i & 1) ? 0xffff0000u : 0x0000ffffu;
        const unsigned m0 = (sp && i < 2) ? half : 0u;
        const unsigned m1 = (sp && i >= 2) ? half : 0u;
        const unsigned o0 = m0 & 0x3f803f80u, o1 = m1 & 0x3f803f80u;
        u32x4 A[4];
        A[0] = u32x4{T0[0] & m0, T0[1] & m1, o0, o1};
        A[1] = u32x4{T0[2] & m0, T0[3] & m1, o0, o1};
        A[2] = u32x4{T1[0] & m0, T1[1] & m1, o0, o1};
        A[3] = u32x4{T1[2] & m0, T1[3] & m1, o0, o1};
        const f32x4 zero = {0.f, 0.f, 0.f, 0.f};
#pragma unroll
        for (int nt = 0; nt < 4; ++nt) {
#pragma unroll
            for (int s = 0; s < 2; ++s) {
                u32x2 d0 = trread(wl + ((2 * s) * 4 + nt) * WBLK), a0 = trread(wl + WIMG + ((2 * s) * 4 + nt) * WBLK);
                u32x2 d1 = trread(wl + ((2 * s + 1) * 4 + nt) * WBLK), a1 = trread(wl + WIMG + ((2 * s + 1) * 4 + nt) * WBLK);
                f32x4 e0 = mfma(A[2 * s], u32x4{d0[0], d0[1], a0[0], a0[1]}, zero);
                f32x4 e1 = mfma(A[2 * s + 1], u32x4{d1[0], d1[1], a1[0], a1[1]}, zero);
                WA[nt][s] = u32x4{pk(e0[0], e0[1]), pk(e0[2], e0[3]), pk(e1[0], e1[1]), pk(e1[2], e1[3])};
            }
        }
    }
    asm volatile("s_waitcnt lgkmcnt(0)" ::: "memory");
    __builtin_amdgcn_s_barrier();
    asm volatile("" ::: "memory");
    STAMP(5);

    f32x4 bias[4];
    {
        LDS_AS char* bp = lds + WBC_OFF + (wave >> 3) * 4 * WBLK + ((wave & 7) * 4 + g) * 16;
#pragma unroll
        for (int nt = 0; nt < 4; ++nt) bias[nt] = *(LDS_AS f32x4*)(bp + nt * WBLK);
    }
    const float b2f = lo16(b2v);
    const f32x4 b2c = {b2f, b2f, b2f, b2f};
    LDS_AS char* rr0 = hreg + c * 128 + ((g ^ (c & 6)) * 16);
    LDS_AS char* rr1 = hreg + c * 128 + (((4 + g) ^ (c & 6)) * 16);
    const unsigned trow = 4 * g + (c >> 2);
    LDS_AS char* tb[4];
#pragma unroll
    for (int dq = 0; dq < 4; ++dq) tb[dq] = hreg + trow * 128 + (((2 * dq) ^ (trow & 6)) * 16) + (c & 3) * 8;
    LDS_AS char* mp = marr + g * 16;

    f32x4 o[4] = {{0.f, 0.f, 0.f, 0.f}, {0.f, 0.f, 0.f, 0.f}, {0.f, 0.f, 0.f, 0.f}, {0.f, 0.f, 0.f, 0.f}};
    asm volatile("s_waitcnt vmcnt(0)" ::: "memory");
    STAMP(6);
    int pass = 0;
    do {
        const int po = pass * 4096;
        unsigned sw[4];
#pragma unroll
        for (int t = 0; t < 2; ++t) {
            u32x4 h0 = *(LDS_AS u32x4*)(rr0 + po + t * 2048);
            u32x4 h1 = *(LDS_AS u32x4*)(rr1 + po + t * 2048);
            f32x4 mm = *(LDS_AS f32x4*)(mp + pass * 128 + t * 64);
            unsigned hp[8];
#pragma unroll
            for (int nt = 0; nt < 4; ++nt) {
                f32x4 z = mfma(WA[nt][0], h0, bias[nt]);
                z = mfma(WA[nt][1], h1, z);
                float s0 = __builtin_amdgcn_rcpf(1.f + __builtin_amdgcn_exp2f(z[0]));
                float s1 = __builtin_amdgcn_rcpf(1.f + __builtin_amdgcn_exp2f(z[1]));
                float s2 = __builtin_amdgcn_rcpf(1.f + __builtin_amdgcn_exp2f(z[2]));
                float s3 = __builtin_amdgcn_rcpf(1.f + __builtin_amdgcn_exp2f(z[3]));
                hp[2 * nt] = pk(s0, s1);
                hp[2 * nt + 1] = pk(s2, s3);
            }
            f32x4 sc = mfma(u32x4{hp[0], hp[1], hp[2], hp[3]}, W20, b2c);
            sc = mfma(u32x4{hp[4], hp[5], hp[6], hp[7]}, W21, sc);
            sw[2 * t] = pk(sc[0] * mm[0], sc[1] * mm[1]);
            sw[2 * t + 1] = pk(sc[2] * mm[2], sc[3] * mm[3]);
        }
        const u32x4 sa = {sw[0], sw[1], sw[2], sw[3]};
#pragma unroll
        for (int dq = 0; dq < 4; ++dq) {
            u32x2 x0 = trread(tb[dq] + po), x1 = trread(tb[dq] + po + 2048);
            o[dq] = mfma(sa, u32x4{x0[0], x0[1], x1[0], x1[1]}, o[dq]);
        }
        ++pass;
    } while (pass * 32 < (int)cnt);
    float r = g == 0 ? o[0][0] : g == 1 ? o[1][0] : g == 2 ? o[2][0] : o[3][0];
    *(unsigned short*)(out + b * 128 + lane * 2) = (unsigned short)pk(r, r);
}

extern "C" void kernel_launch(void* const* d_in, const int* in_sizes, int n_in,
                              void* d_out, int out_size, void* d_ws, size_t ws_size,
                              hipStream_t stream) {
    const int B = in_sizes[0] / 64;
    din_kernel<<<B / NW, NW * 64, 0, stream>>>((const char*)d_in[0], (const char*)d_in[1], (const char*)d_in[2],
                                               (const char*)d_in[3], (const char*)d_in[4], (const char*)d_in[5],
                                               (const char*)d_in[6], (char*)d_out
    );
}
```

```cpp
#include <hip/hip_runtime.h>

typedef __bf16 bf16x8 __attribute__((ext_vector_type(8)));
typedef __bf16 bf16x2 __attribute__((ext_vector_type(2)));
typedef short s16x4 __attribute__((ext_vector_type(4)));
typedef float f32x4 __attribute__((ext_vector_type(4)));
typedef float f32x2 __attribute__((ext_vector_type(2)));
typedef unsigned u32x4 __attribute__((ext_vector_type(4)));
typedef unsigned u32x2 __attribute__((ext_vector_type(2)));
#define LDS_AS __attribute__((address_space(3)))
#define DI static __device__ __forceinline__

constexpr int LH = 50;
constexpr int NW = 16;
constexpr int HREG = 8192;
constexpr int WBLK = 544;
constexpr int WIMG = 16 * WBLK;
constexpr int WD_OFF = NW * HREG;
constexpr int WAC_OFF = WD_OFF + WIMG;
constexpr int WBC_OFF = WAC_OFF + WIMG;
constexpr int M_OFF = WBC_OFF + WIMG;
constexpr int LDS_TOTAL = M_OFF + NW * 256;

DI unsigned pk(float a, float b) {
    f32x2 p = {a, b};
    return __builtin_bit_cast(unsigned, __builtin_convertvector(p, bf16x2));
}
DI f32x4 mfma(u32x4 a, u32x4 b, f32x4 c) {
    return __builtin_amdgcn_mfma_f32_16x16x32_bf16(__builtin_bit_cast(bf16x8, a), __builtin_bit_cast(bf16x8, b), c, 0, 0, 0);
}
DI u32x2 trread(LDS_AS char* p) {
    return __builtin_bit_cast(u32x2, __builtin_amdgcn_ds_read_tr16_b64_v4i16((LDS_AS s16x4*)p));
}
DI float lo16(unsigned u) { return __uint_as_float(u << 16); }
DI float hi16(unsigned u) { return __uint_as_float(u & 0xffff0000u); }

#define STAMP(i)
#define RSTAMP(i)

#define HNT " nt"

__global__ __launch_bounds__(1024) void din_kernel(const char* __restrict__ tgt, const char* __restrict__ hist,
                                                   const char* __restrict__ mask, const char* __restrict__ w1,
                                                   const char* __restrict__ b1, const char* __restrict__ w2,
                                                   const char* __restrict__ b2, char* __restrict__ out
) {
    __shared__ __attribute__((aligned(16))) char lds_[LDS_TOTAL];
    LDS_AS char* lds = (LDS_AS char*)lds_;
    const unsigned tid = threadIdx.x, lane = tid & 63;
    const unsigned wave = __builtin_amdgcn_readfirstlane(tid >> 6);
    const unsigned b0 = blockIdx.x * NW;
    const unsigned b = b0 + wave;
    const unsigned c = lane & 15, g = lane >> 4;

    unsigned mv;
    {
        unsigned off = b * (LH * 2) + (lane < LH ? lane : LH - 1) * 2;
        asm volatile("global_load_ushort %0, %1, %2" : "=v"(mv) : "v"(off), "s"(mask));
    }
    u32x2 wa, wb, wc, wd;
    {
        unsigned off = tid * 8;
        unsigned off2 = off + 16384;
        asm volatile("global_load_dwordx2 %0, %4, %6\n\t"
                     "global_load_dwordx2 %2, %5, %6\n\t"
                     "global_load_dwordx2 %1, %4, %7\n\t"
                     "global_load_dwordx2 %3, %5, %7"
                     : "=&v"(wa), "=&v"(wb), "=&v"(wc), "=&v"(wd)
                     : "v"(off), "v"(off2), "s"(w1), "s"(w1 + 8192));
    }
    u32x4 T0, T1, W20, W21;
    unsigned b2v;
    {
        unsigned off = g * 16;
        unsigned offt = off + b * 128;
        unsigned z = 0;
        asm volatile("global_load_dwordx4 %0, %5, %8\n\t"
                     "global_load_dwordx4 %1, %5, %8 offset:64\n\t"
                     "global_load_dwordx4 %2, %6, %9\n\t"
                     "global_load_dwordx4 %3, %6, %9 offset:64\n\t"
                     "global_load_ushort %4, %7, %10"
                     : "=&v"(T0), "=&v"(T1), "=&v"(W20), "=&v"(W21), "=&v"(b2v)
                     : "v"(offt), "v"(off), "v"(z), "s"(tgt), "s"(w2), "s"(b2));
    }
    u32x4 X0, X1;
    u32x2 b1v;
    if (wave < 4) {
        unsigned offx = (b0 + c) * 128 + g * 16;
        unsigned offb = (wave >> 1) * 64 + g * 16 + (wave & 1) * 8;
        asm volatile("global_load_dwordx4 %0, %3, %5\n\t"
                     "global_load_dwordx4 %1, %3, %5 offset:64\n\t"
                     "global_load_dwordx2 %2, %4, %6\n\t"
                     "s_waitcnt vmcnt(12)"
                     : "=&v"(X0), "=&v"(X1), "=&v"(b1v)
                     : "v"(offx), "v"(offb), "s"(tgt), "s"(b1));
    } else {
        asm volatile("s_waitcnt vmcnt(9)" : "=v"(X0), "=v"(X1), "=v"(b1v));
    }
    asm volatile("" : "+v"(mv));
    STAMP(2);

    const bool set = (lane < LH) && ((mv & 0x7fffu) != 0);
    const unsigned long long M = __ballot(set);
    const unsigned rank = __builtin_amdgcn_mbcnt_hi((unsigned)(M >> 32), __builtin_amdgcn_mbcnt_lo((unsigned)M, 0));
    const unsigned cnt = __builtin_popcountll(M);
    const unsigned dest = set ? rank : cnt + lane - rank;
    LDS_AS char* hreg = lds + wave * HREG;
    LDS_AS char* marr = lds + M_OFF + wave * 256;
    *(LDS_AS unsigned*)(hreg + 62 * 128 + ((dest & 7) * 8 + (dest >> 3)) * 4) = set ? lane * 128 : 0x40000000u;
    *(LDS_AS unsigned*)(marr + dest * 4) = set ? mv << 16 : 0u;
    asm volatile("" ::: "memory");
    u32x4 ia = *(LDS_AS u32x4*)(hreg + 62 * 128 + (lane >> 3) * 32);
    u32x4 ib = *(LDS_AS u32x4*)(hreg + 62 * 128 + (lane >> 3) * 32 + 16);
    const unsigned choff = ((lane & 7) ^ ((lane >> 3) & 6)) * 16;
    u32x4 srd;
    {
        unsigned long long hb = (unsigned long long)hist + (unsigned long long)b * (LH * 128);
        srd[0] = (unsigned)hb;
        srd[1] = (unsigned)(hb >> 32) & 0xffffu;
        srd[2] = LH * 128;
        srd[3] = 0x00020000u;
    }
    const unsigned hbase = (unsigned)(size_t)hreg;
    ia += choff;
    asm volatile("s_mov_b32 m0, %0\n\t"
                 "s_nop 0\n\t"
                 "buffer_load_dwordx4 %1, %5, 0 offen" HNT " lds" "\n\t"
                 "s_add_u32 m0, m0, 0x400\n\t"
                 "s_nop 0\n\t"
                 "buffer_load_dwordx4 %2, %5, 0 offen" HNT " lds" "\n\t"
                 "s_add_u32 m0, m0, 0x400\n\t"
                 "s_nop 0\n\t"
                 "buffer_load_dwordx4 %3, %5, 0 offen" HNT " lds" "\n\t"
                 "s_add_u32 m0, m0, 0x400\n\t"
                 "s_nop 0\n\t"
                 "buffer_load_dwordx4 %4, %5, 0 offen" HNT " lds"
                 :: "s"(hbase), "v"(ia[0]), "v"(ia[1]), "v"(ia[2]), "v"(ia[3]), "s"(srd) : "memory", "scc");
    if (cnt > 32) {
        ib += choff;
        asm volatile("s_add_u32 m0, %0, 0x1000\n\t"
                     "s_nop 0\n\t"
                     "buffer_load_dwordx4 %1, %5, 0 offen" HNT " lds" "\n\t"
                     "s_add_u32 m0, m0, 0x400\n\t"
                     "s_nop 0\n\t"
                     "buffer_load_dwordx4 %2, %5, 0 offen" HNT " lds" "\n\t"
                     "s_add_u32 m0, m0, 0x400\n\t"
                     "s_nop 0\n\t"
                     "buffer_load_dwordx4 %3, %5, 0 offen" HNT " lds" "\n\t"
                     "s_add_u32 m0, m0, 0x400\n\t"
                     "s_nop 0\n\t"
                     "buffer_load_dwordx4 %4, %5, 0 offen" HNT " lds" "\n\t"
                     "s_waitcnt vmcnt(8)"
                     :: "s"(hbase), "v"(ib[0]), "v"(ib[1]), "v"(ib[2]), "v"(ib[3]), "s"(srd) : "memory", "scc");
    } else {
        asm volatile("s_waitcnt vmcnt(4)" ::: "memory");
    }
    asm volatile("" : "+v"(wa), "+v"(wb), "+v"(wc), "+v"(wd), "+v"(T0), "+v"(T1), "+v"(W20), "+v"(W21), "+v"(b2v),
                      "+v"(X0), "+v"(X1), "+v"(b1v));
    STAMP(3);

    const float KS = -1.4426950408889634f;
    {
        float a[4] = {lo16(wa[0]), hi16(wa[0]), lo16(wa[1]), hi16(wa[1])};
        float bb[4] = {lo16(wb[0]), hi16(wb[0]), lo16(wb[1]), hi16(wb[1])};
        float cc[4] = {lo16(wc[0]), hi16(wc[0]), lo16(wc[1]), hi16(wc[1])};
        float dd[4] = {lo16(wd[0]), hi16(wd[0]), lo16(wd[1]), hi16(wd[1])};
        float ac[4], bc[4], dk[4];
#pragma unroll
        for (int i = 0; i < 4; ++i) {
            float ck = cc[i] * KS;
            ac[i] = __builtin_fmaf(a[i], KS, ck);
            bc[i] = __builtin_fmaf(bb[i], KS, -ck);
            dk[i] = dd[i] * KS;
        }
        const unsigned d = tid >> 4;
        const unsigned dt = 2 * (d >> 5) + ((d >> 2) & 1), gd = (d >> 3) & 3, q = d & 3;
        const unsigned nt = 2 * ((tid >> 3) & 1) + (tid & 1), p = (tid >> 1) & 3;
        LDS_AS char* wp = lds + WD_OFF + (dt * 4 + nt) * WBLK + gd * 128 + q * 32 + p * 8;
        *(LDS_AS u32x2*)(wp) = u32x2{pk(dk[0], dk[1]), pk(dk[2], dk[3])};
        *(LDS_AS u32x2*)(wp + WIMG) = u32x2{pk(ac[0], ac[1]), pk(ac[2], ac[3])};
        *(LDS_AS u32x2*)(wp + 2 * WIMG) = u32x2{pk(bc[0], bc[1]), pk(bc[2], bc[3])};
    }
    asm volatile("s_waitcnt lgkmcnt(0)" ::: "memory");
    __builtin_amdgcn_s_barrier();
    asm volatile("" ::: "memory");
    STAMP(4);

    LDS_AS char* wl = lds + WD_OFF + lane * 8;
    if (wave < 4) {
        LDS_AS char* wq = wl + 2 * WIMG + wave * WBLK;
        u32x2 f00 = trread(wq), f01 = trread(wq + 4 * WBLK), f10 = trread(wq + 8 * WBLK), f11 = trread(wq + 12 * WBLK);
        f32x4 acc = {lo16(b1v[0]) * KS, hi16(b1v[0]) * KS, lo16(b1v[1]) * KS, hi16(b1v[1]) * KS};
        acc = mfma(u32x4{f00[0], f00[1], f01[0], f01[1]}, X0, acc);
        acc = mfma(u32x4{f10[0], f10[1], f11[0], f11[1]}, X1, acc);
        *(LDS_AS f32x4*)(lds + WBC_OFF + ((c >> 3) * 4 + wave) * WBLK + ((c & 7) * 4 + g) * 16) = acc;
    }

    u32x4 WA[4][2];
    {
        const bool sp = (c >> 2) == g;
        const unsigned i = c & 3;
        const unsigned half = (i & 1) ? 0xffff0000u : 0x0000ffffu;
        const unsigned m0 = (sp && i < 2) ? half : 0u;
        const unsigned m1 = (sp && i >= 2) ? half : 0u;
        const unsigned o0 = m0 & 0x3f803f80u, o1 = m1 & 0x3f803f80u;
        u32x4 A[4];
        A[0] = u32x4{T0[0] & m0, T0[1] & m1, o0, o1};
        A[1] = u32x4{T0[2] & m0, T0[3] & m1, o0, o1};
        A[2] = u32x4{T1[0] & m0, T1[1] & m1, o0, o1};
        A[3] = u32x4{T1[2] & m0, T1[3] & m1, o0, o1};
        const f32x4 zero = {0.f, 0.f, 0.f, 0.f};
#pragma unroll
        for (int nt = 0; nt < 4; ++nt) {
#pragma unroll
            for (int s = 0; s < 2; ++s) {
                u32x2 d0 = trread(wl + ((2 * s) * 4 + nt) * WBLK), a0 = trread(wl + WIMG + ((2 * s) * 4 + nt) * WBLK);
                u32x2 d1 = trread(wl + ((2 * s + 1) * 4 + nt) * WBLK), a1 = trread(wl + WIMG + ((2 * s + 1) * 4 + nt) * WBLK);
                f32x4 e0 = mfma(A[2 * s], u32x4{d0[0], d0[1], a0[0], a0[1]}, zero);
                f32x4 e1 = mfma(A[2 * s + 1], u32x4{d1[0], d1[1], a1[0], a1[1]}, zero);
                WA[nt][s] = u32x4{pk(e0[0], e0[1]), pk(e0[2], e0[3]), pk(e1[0], e1[1]), pk(e1[2], e1[3])};
            }
        }
    }
    asm volatile("s_waitcnt lgkmcnt(0)" ::: "memory");
    __builtin_amdgcn_s_barrier();
    asm volatile("" ::: "memory");
    STAMP(5);

    f32x4 bias[4];
    {
        LDS_AS char* bp = lds + WBC_OFF + (wave >> 3) * 4 * WBLK + ((wave & 7) * 4 + g) * 16;
#pragma unroll
        for (int nt = 0; nt < 4; ++nt) bias[nt] = *(LDS_AS f32x4*)(bp + nt * WBLK);
    }
    const float b2f = lo16(b2v);
    const f32x4 b2c = {b2f, b2f, b2f, b2f};
    LDS_AS char* rr0 = hreg + c * 128 + ((g ^ (c & 6)) * 16);
    LDS_AS char* rr1 = hreg + c * 128 + (((4 + g) ^ (c & 6)) * 16);
    const unsigned trow = 4 * g + (c >> 2);
    LDS_AS char* tb[4];
#pragma unroll
    for (int dq = 0; dq < 4; ++dq) tb[dq] = hreg + trow * 128 + (((2 * dq) ^ (trow & 6)) * 16) + (c & 3) * 8;
    LDS_AS char* mp = marr + g * 16;

    f32x4 o[4] = {{0.f, 0.f, 0.f, 0.f}, {0.f, 0.f, 0.f, 0.f}, {0.f, 0.f, 0.f, 0.f}, {0.f, 0.f, 0.f, 0.f}};
    asm volatile("s_waitcnt vmcnt(0)" ::: "memory");
    STAMP(6);
    int pass = 0;
    do {
        const int po = pass * 4096;
        unsigned sw[4];
#pragma unroll
        for (int t = 0; t < 2; ++t) {
            u32x4 h0 = *(LDS_AS u32x4*)(rr0 + po + t * 2048);
            u32x4 h1 = *(LDS_AS u32x4*)(rr1 + po + t * 2048);
            f32x4 mm = *(LDS_AS f32x4*)(mp + pass * 128 + t * 64);
            unsigned hp[8];
#pragma unroll
            for (int nt = 0; nt < 4; ++nt) {
                f32x4 z = mfma(WA[nt][0], h0, bias[nt]);
                z = mfma(WA[nt][1], h1, z);
                float s0 = __builtin_amdgcn_rcpf(1.f + __builtin_amdgcn_exp2f(z[0]));
                float s1 = __builtin_amdgcn_rcpf(1.f + __builtin_amdgcn_exp2f(z[1]));
                float s2 = __builtin_amdgcn_rcpf(1.f + __builtin_amdgcn_exp2f(z[2]));
                float s3 = __builtin_amdgcn_rcpf(1.f + __builtin_amdgcn_exp2f(z[3]));
                hp[2 * nt] = pk(s0, s1);
                hp[2 * nt + 1] = pk(s2, s3);
            }
            f32x4 sc = mfma(u32x4{hp[0], hp[1], hp[2], hp[3]}, W20, b2c);
            sc = mfma(u32x4{hp[4], hp[5], hp[6], hp[7]}, W21, sc);
            sw[2 * t] = pk(sc[0] * mm[0], sc[1] * mm[1]);
            sw[2 * t + 1] = pk(sc[2] * mm[2], sc[3] * mm[3]);
        }
        const u32x4 sa = {sw[0], sw[1], sw[2], sw[3]};
#pragma unroll
        for (int dq = 0; dq < 4; ++dq) {
            u32x2 x0 = trread(tb[dq] + po), x1 = trread(tb[dq] + po + 2048);
            o[dq] = mfma(sa, u32x4{x0[0], x0[1], x1[0], x1[1]}, o[dq]);
        }
        ++pass;
    } while (pass * 32 < (int)cnt);
    float r = g == 0 ? o[0][0] : g == 1 ? o[1][0] : g == 2 ? o[2][0] : o[3][0];
    *(unsigned short*)(out + b * 128 + lane * 2) = (unsigned short)pk(r, r);
}

extern "C" void kernel_launch(void* const* d_in, const int* in_sizes, int n_in,
                              void* d_out, int out_size, void* d_ws, size_t ws_size,
                              hipStream_t stream) {
    const int B = in_sizes[0] / 64;
    din_kernel<<<B / NW, NW * 64, 0, stream>>>((const char*)d_in[0], (const char*)d_in[1], (const char*)d_in[2],
                                               (const char*)d_in[3], (const char*)d_in[4], (const char*)d_in[5],
                                               (const char*)d_in[6], (char*)d_out
    );
}
```

```cpp
#include <hip/hip_runtime.h>

typedef __bf16 bf16x8 __attribute__((ext_vector_type(8)));
typedef __bf16 bf16x2 __attribute__((ext_vector_type(2)));
typedef short s16x4 __attribute__((ext_vector_type(4)));
typedef float f32x4 __attribute__((ext_vector_type(4)));
typedef float f32x2 __attribute__((ext_vector_type(2)));
typedef unsigned u32x4 __attribute__((ext_vector_type(4)));
typedef unsigned u32x2 __attribute__((ext_vector_type(2)));
#define LDS_AS __attribute__((address_space(3)))
#define DI static __device__ __forceinline__
typedef LDS_AS char* lptr;

constexpr int LH = 50;
constexpr int NW = 8;
constexpr int NB = 16;
constexpr int W_OFF = 0;
constexpr int T_OFF = 32768;
constexpr int S_OFF = T_OFF + 2048;
constexpr int BP = 272;
constexpr int B_OFF = S_OFF + 1024;
constexpr int M_OFF = B_OFF + NB * BP;
constexpr int H_OFF = M_OFF + NB * 512;
constexpr int HREG = 56 * 128;
constexpr int LDS_TOTAL = H_OFF + NB * HREG;
static_assert(LDS_TOTAL <= 163840 && H_OFF % 256 == 0, "lds");

DI unsigned pk(float a, float b) {
    f32x2 p = {a, b};
    return __builtin_bit_cast(unsigned, __builtin_convertvector(p, bf16x2));
}
DI f32x4 mfma(u32x4 a, u32x4 b, f32x4 c) {
    return __builtin_amdgcn_mfma_f32_16x16x32_bf16(__builtin_bit_cast(bf16x8, a), __builtin_bit_cast(bf16x8, b), c, 0, 0, 0);
}
DI u32x2 trread(lptr p) {
    return __builtin_bit_cast(u32x2, __builtin_amdgcn_ds_read_tr16_b64_v4i16((LDS_AS s16x4*)p));
}
DI u32x4 ld16(lptr p) { return *(LDS_AS u32x4*)p; }
DI f32x4 ld16f(lptr p) { return *(LDS_AS f32x4*)p; }
DI u32x2 ld8(lptr p) { return *(LDS_AS u32x2*)p; }
DI u32x4 cat(u32x2 a, u32x2 b) { return u32x4{a[0], a[1], b[0], b[1]}; }
DI float lo16(unsigned u) { return __uint_as_float(u << 16); }
DI float hi16(unsigned u) { return __uint_as_float(u & 0xffff0000u); }
DI float ex2(float x) { return __builtin_amdgcn_exp2f(x); }
DI float rcp(float x) { return __builtin_amdgcn_rcpf(x); }
#define PIN(x) asm volatile("" : "+v"(x))
#define SB() __builtin_amdgcn_sched_barrier(0)
#define BARRIER() do { asm volatile("s_waitcnt lgkmcnt(0)" ::: "memory"); __builtin_amdgcn_s_barrier(); asm volatile("" ::: "memory"); } while (0)

#define STAMP(i)
#define RSTAMP(i)

#define HNT " nt"

DI void dma4(unsigned ldsbase, u32x4 o, u32x4 srd) {
    asm volatile("s_mov_b32 m0, %0\n\t"
                 "s_nop 0\n\t"
                 "buffer_load_dwordx4 %1, %5, 0 offen" HNT " lds\n\t"
                 "s_add_u32 m0, m0, 0x400\n\t"
                 "s_nop 0\n\t"
                 "buffer_load_dwordx4 %2, %5, 0 offen" HNT " lds\n\t"
                 "s_add_u32 m0, m0, 0x400\n\t"
                 "s_nop 0\n\t"
                 "buffer_load_dwordx4 %3, %5, 0 offen" HNT " lds\n\t"
                 "s_add_u32 m0, m0, 0x400\n\t"
                 "s_nop 0\n\t"
                 "buffer_load_dwordx4 %4, %5, 0 offen" HNT " lds"
                 :: "s"(ldsbase), "v"(o[0]), "v"(o[1]), "v"(o[2]), "v"(o[3]), "s"(srd) : "memory", "scc");
}
DI void dma3(unsigned ldsbase, u32x4 o, u32x4 srd) {
    asm volatile("s_mov_b32 m0, %0\n\t"
                 "s_nop 0\n\t"
                 "buffer_load_dwordx4 %1, %4, 0 offen" HNT " lds\n\t"
                 "s_add_u32 m0, m0, 0x400\n\t"
                 "s_nop 0\n\t"
                 "buffer_load_dwordx4 %2, %4, 0 offen" HNT " lds\n\t"
                 "s_add_u32 m0, m0, 0x400\n\t"
                 "s_nop 0\n\t"
                 "buffer_load_dwordx4 %3, %4, 0 offen" HNT " lds"
                 :: "s"(ldsbase), "v"(o[0]), "v"(o[1]), "v"(o[2]), "s"(srd) : "memory", "scc");
}

struct Addr { lptr rr0, rr1, tb[4], mp, bp; };

DI f32x4 score(const f32x4 (&z)[4], const u32x4& W20, const u32x4& W21, const f32x4& b2c) {
    unsigned hp[8];
#pragma unroll
    for (int nt = 0; nt < 4; ++nt) {
        f32x4 t = {ex2(z[nt][0]), ex2(z[nt][1]), ex2(z[nt][2]), ex2(z[nt][3])};
        t += 1.f;
        hp[2 * nt] = pk(rcp(t[0]), rcp(t[1]));
        hp[2 * nt + 1] = pk(rcp(t[2]), rcp(t[3]));
    }
    f32x4 sc = mfma(u32x4{hp[0], hp[1], hp[2], hp[3]}, W20, b2c);
    return mfma(u32x4{hp[4], hp[5], hp[6], hp[7]}, W21, sc);
}

DI float pick(const f32x4 (&o)[4], unsigned lane) {
    float o0 = o[0][0], o1 = o[1][0], o2 = o[2][0], o3 = o[3][0];
    PIN(o0); PIN(o1); PIN(o2); PIN(o3);
    const float r01 = (lane & 16) ? o1 : o0;
    const float r23 = (lane & 16) ? o3 : o2;
    return (lane & 32) ? r23 : r01;
}

template <int J>
DI float run_batch(const Addr& a, unsigned lane, int cnt, const u32x4 (&WA)[4][2], const u32x4& W20, const u32x4& W21, const f32x4& b2c) {
    constexpr int HO = J * HREG, MO = J * 512, BO = J * BP;
    const f32x4 zero = {0.f, 0.f, 0.f, 0.f};
    f32x4 bias[4];
#pragma unroll
    for (int nt = 0; nt < 4; ++nt) bias[nt] = ld16f(a.bp + BO + nt * 64);
    float r = 0.f;
    if (__builtin_expect(cnt > 32, 0)) {
        for (int row = 32; row < cnt; row += 16) {
            const int ro = row == 32 ? 32 : 40;
            const u32x4 h0 = ld16(a.rr0 + HO + ro * 128), h1 = ld16(a.rr1 + HO + ro * 128);
            f32x4 mm = ld16f(a.mp + MO + ro * 4);
            if (row != 32 && lane < 32) mm = zero;
            f32x4 z[4];
#pragma unroll
            for (int nt = 0; nt < 4; ++nt) z[nt] = mfma(WA[nt][1], h1, mfma(WA[nt][0], h0, bias[nt]));
            const f32x4 sc = score(z, W20, W21, b2c) * mm;
            const u32x4 sa = {pk(sc[0], sc[1]), pk(sc[2], sc[3]), 0u, 0u};
            f32x4 o[4];
#pragma unroll
            for (int dq = 0; dq < 4; ++dq) { const u32x2 x = trread(a.tb[dq] + HO + ro * 128); o[dq] = mfma(sa, cat(x, x), zero); }
            r += pick(o, lane);
        }
    }
    const u32x4 h00 = ld16(a.rr0 + HO), h01 = ld16(a.rr1 + HO);
    const u32x4 h10 = ld16(a.rr0 + HO + 2048), h11 = ld16(a.rr1 + HO + 2048);
    const f32x4 mm0 = ld16f(a.mp + MO), mm1 = ld16f(a.mp + MO + 64);
    u32x4 xb[4];
#pragma unroll
    for (int dq = 0; dq < 4; ++dq) xb[dq] = cat(trread(a.tb[dq] + HO), trread(a.tb[dq] + HO + 2048));
    SB();
    f32x4 z0[4], z1[4];
#pragma unroll
    for (int nt = 0; nt < 4; ++nt) z0[nt] = mfma(WA[nt][0], h00, bias[nt]);
#pragma unroll
    for (int nt = 0; nt < 4; ++nt) z0[nt] = mfma(WA[nt][1], h01, z0[nt]);
    SB();
#pragma unroll
    for (int nt = 0; nt < 4; ++nt) {
        z1[nt] = mfma(WA[nt][0], h10, bias[nt]);
        SB();
        z0[nt >> 1][(nt & 1) * 2] = ex2(z0[nt >> 1][(nt & 1) * 2]);
        z0[nt >> 1][(nt & 1) * 2 + 1] = ex2(z0[nt >> 1][(nt & 1) * 2 + 1]);
        SB();
    }
#pragma unroll
    for (int nt = 0; nt < 4; ++nt) {
        z1[nt] = mfma(WA[nt][1], h11, z1[nt]);
        SB();
        z0[2 + (nt >> 1)][(nt & 1) * 2] = ex2(z0[2 + (nt >> 1)][(nt & 1) * 2]);
        z0[2 + (nt >> 1)][(nt & 1) * 2 + 1] = ex2(z0[2 + (nt >> 1)][(nt & 1) * 2 + 1]);
        SB();
    }
    unsigned hp[8];
#pragma unroll
    for (int nt = 0; nt < 4; ++nt) {
        f32x4 t = z0[nt] + 1.f;
        hp[2 * nt] = pk(rcp(t[0]), rcp(t[1]));
        hp[2 * nt + 1] = pk(rcp(t[2]), rcp(t[3]));
    }
    f32x4 sc0 = mfma(u32x4{hp[0], hp[1], hp[2], hp[3]}, W20, b2c);
    sc0 = mfma(u32x4{hp[4], hp[5], hp[6], hp[7]}, W21, sc0);
    PIN(sc0);
    f32x4 sc1 = score(z1, W20, W21, b2c);
    sc0 *= mm0;
    sc1 *= mm1;
    const u32x4 sa = {pk(sc0[0], sc0[1]), pk(sc0[2], sc0[3]), pk(sc1[0], sc1[1]), pk(sc1[2], sc1[3])};
    f32x4 o[4];
#pragma unroll
    for (int dq = 0; dq < 4; ++dq) o[dq] = mfma(sa, xb[dq], zero);
    return r + pick(o, lane);
}

DI int compact(unsigned mv, unsigned lane, unsigned rowoff, lptr marr) {
    const unsigned long long M = __builtin_amdgcn_ballot_w64((mv & 0x7fffu) != 0) & ((1ull << LH) - 1);
    const bool set = __builtin_amdgcn_inverse_ballot_w64(M);
    const unsigned rank = __builtin_amdgcn_mbcnt_hi((unsigned)(M >> 32), __builtin_amdgcn_mbcnt_lo((unsigned)M, 0));
    const int cnt = __builtin_popcount((unsigned)M) + __builtin_popcount((unsigned)(M >> 32));
    const unsigned dest = set ? rank : cnt + lane - rank;
    *(LDS_AS unsigned*)(marr + 256 + ((dest & 7) * 8 + (dest >> 3)) * 4) = set ? rowoff : 0x40000000u;
    *(LDS_AS unsigned*)(marr + dest * 4) = set ? mv << 16 : 0u;
    return cnt;
}

__global__ __launch_bounds__(NW * 64) void din_kernel(const char* __restrict__ tgt, const char* __restrict__ hist,
                                                      const char* __restrict__ mask, const char* __restrict__ w1,
                                                      const char* __restrict__ b1, const char* __restrict__ w2,
                                                      const char* __restrict__ b2, char* __restrict__ out
) {
    __shared__ __attribute__((aligned(256))) char lds_[LDS_TOTAL];
    lptr lds = (lptr)lds_;
    const unsigned lbase = (unsigned)(size_t)lds;
    const unsigned tid = threadIdx.x, lane = tid & 63;
    const unsigned wave = __builtin_amdgcn_readfirstlane(tid >> 6);
    const unsigned b0 = blockIdx.x * NB;
    const unsigned b = b0 + wave * 2;
    const unsigned c = lane & 15, g = lane >> 4;
    const unsigned oct = lane >> 3, pos = lane & 7;

    asm volatile("" :: "s"(tgt), "s"(hist), "s"(mask), "s"(w1), "s"(b1), "s"(w2), "s"(b2), "s"(out));
    unsigned mvA, mvB;
    {
        unsigned off = b * (LH * 2) + (lane < LH ? lane : LH - 1) * 2;
        asm volatile("global_load_ushort %0, %2, %3\n\tglobal_load_ushort %1, %2, %3 offset:100"
                     : "=&v"(mvA), "=&v"(mvB) : "v"(off), "s"(mask));
    }
    unsigned choff = (pos ^ (oct & 6)) * 16;
    if (wave < 2) {
        unsigned off = (b0 + wave * 8 + oct) * 128 + choff;
        asm volatile("s_mov_b32 m0, %0\n\ts_nop 0\n\tglobal_load_lds_dwordx4 %1, %2"
                     :: "s"(lbase + T_OFF + wave * 1024), "v"(off), "s"(tgt) : "memory");
    } else if (wave == 2) {
        const char* p = (lane < 32 ? w2 : b1) + (lane & 31) * 4;
        asm volatile("s_mov_b32 m0, %0\n\ts_nop 0\n\tglobal_load_lds_dword %1, off"
                     :: "s"(lbase + S_OFF), "v"(p) : "memory");
    } else if (wave == 3) {
        unsigned off = 0;
        asm volatile("s_mov_b32 m0, %0\n\ts_nop 0\n\tglobal_load_lds_ushort %1, %2"
                     :: "s"(lbase + S_OFF + 256), "v"(off), "s"(b2) : "memory");
    }
    {
        unsigned o0 = wave * 4096 + oct * 128 + ((pos ^ (oct & 2)) * 16);
        unsigned o1 = o0 ^ 64;
        asm volatile("s_mov_b32 m0, %0\n\ts_nop 0\n\t"
                     "global_load_lds_dwordx4 %1, %3\n\t"
                     "global_load_lds_dwordx4 %2, %3 offset:1024\n\t"
                     "global_load_lds_dwordx4 %1, %3 offset:2048\n\t"
                     "global_load_lds_dwordx4 %2, %3 offset:3072"
                     :: "s"(lbase + W_OFF + wave * 4096), "v"(o0), "v"(o1), "s"(w1) : "memory");
    }

    const unsigned q = c >> 2, p = c & 3;
    Addr a;
    {
        lptr hreg = lds + H_OFF + wave * (2 * HREG);
        a.rr0 = hreg + c * 128 + ((g ^ (c & 6)) * 16);
        a.rr1 = hreg + c * 128 + (((4 + g) ^ (c & 6)) * 16);
        const unsigned trow = 4 * g + q;
#pragma unroll
        for (int dq = 0; dq < 4; ++dq) { a.tb[dq] = hreg + trow * 128 + (((2 * dq) ^ (trow & 6)) * 16) + p * 8; PIN(a.tb[dq]); }
        a.mp = lds + M_OFF + wave * 1024 + g * 16;
        a.bp = lds + B_OFF + wave * (2 * BP) + g * 16;
        PIN(a.rr0); PIN(a.rr1); PIN(a.mp); PIN(a.bp);
    }
    const unsigned fa = (g & 1) * 24576 + (16 * (lane >> 5) + 8 * ((lane >> 2) & 1) + (lane & 2) + (oct & 1)) * 128 + pos * 16;
    lptr wb[4];
#pragma unroll
    for (int nt = 0; nt < 4; ++nt) { wb[nt] = lds + W_OFF + (fa ^ (nt * 32)); PIN(wb[nt]); }
    const unsigned f = ((q >> 1) & 1) | ((g & 1) << 1);
    lptr mini = lds + W_OFF + (wave >> 2) * 4096 + (8 * g + q) * 128 + (((wave & 3) ^ f) * 32) + p * 8;
    lptr minw = lds + W_OFF + (wave >> 2) * 4096 + (fa ^ ((wave & 3) * 32));
    lptr tp0 = lds + T_OFF + wave * 256 + ((g ^ ((2 * wave) & 6)) * 16);
    lptr tp1 = lds + T_OFF + wave * 256 + (((4 + g) ^ ((2 * wave) & 6)) * 16);
    lptr sp8 = lds + S_OFF + g * 8;
    const unsigned half = (p & 1) ? 0xffff0000u : 0x0000ffffu;
    unsigned m0 = (q == g && p < 2) ? half : 0u;
    unsigned m1 = (q == g && p >= 2) ? half : 0u;
    PIN(mini); PIN(minw); PIN(tp0); PIN(tp1); PIN(sp8); PIN(m0); PIN(m1);
    const unsigned KH = 0xbfb9bfb9u, KL = 0x3b2c3b2cu, ONE = 0x3f803f80u;
    u32x4 selH = {m0 & KH, m1 & KH, m0 & KH, m1 & KH};
    u32x4 selL = {m0 & KL, m1 & KL, m0 & KL, m1 & KL};
    u32x4 selD = {m0 & KH, m1 & KH, m0 & KL, m1 & KL};
    u32x4 A[2][4];
#pragma unroll
    for (int i = 0; i < 8; ++i) { A[i >> 2][i & 3][2] = m0 & ONE; A[i >> 2][i & 3][3] = m1 & ONE; PIN(A[i >> 2][i & 3]); }
    PIN(selH); PIN(selL); PIN(selD);
    lptr marr = lds + M_OFF + wave * 1024;
    lptr ip = marr + 256 + oct * 32;
    unsigned ooff = b * 128 + lane * 2;
    unsigned l128 = lane * 128, l128b = lane * 128 + LH * 128;
    PIN(ip); PIN(choff); PIN(ooff); PIN(l128); PIN(l128b);
    u32x4 srd;
    {
        unsigned long long hb = (unsigned long long)hist + (unsigned long long)b * (LH * 128);
        srd[0] = (unsigned)hb;
        srd[1] = (unsigned)(hb >> 32) & 0xffffu;
        srd[2] = 2 * LH * 128;
        srd[3] = 0x00020000u;
    }
    const unsigned hbase = lbase + H_OFF + wave * (2 * HREG);
    asm volatile("" :: "s"(srd), "s"(hbase));

    asm volatile("s_waitcnt vmcnt(4)" : "+v"(mvA), "+v"(mvB));
    STAMP(2);

    const int cntA = compact(mvA, lane, l128, marr);
    asm volatile("" ::: "memory");
    u32x4 ia = ld16(ip);
    const int cntB = compact(mvB, lane, l128b, marr + 512);
    asm volatile("" ::: "memory");
    u32x4 ib = ld16(ip + 512);
    dma4(hbase, ia + choff, srd);
    asm volatile("s_waitcnt vmcnt(4)" ::: "memory");
    ib += choff;
    PIN(ib);
    STAMP(3);
    BARRIER();
    STAMP(4);

    const f32x4 zero = {0.f, 0.f, 0.f, 0.f};
    {
        const u32x2 wa0 = trread(mini), wc0 = trread(mini + 16384), wd0 = trread(mini + 24576);
        const u32x2 wa1 = trread(mini + 512), wc1 = trread(mini + 16384 + 512), wd1 = trread(mini + 24576 + 512);
        const u32x4 TA0 = ld16(tp0), TA1 = ld16(tp1), TB0 = ld16(tp0 + 128), TB1 = ld16(tp1 + 128);
        f32x4 ac0 = mfma(selH, cat(wa0, wc0), zero);
        f32x4 ac1 = mfma(selH, cat(wa1, wc1), zero);
        f32x4 dk0 = mfma(selD, cat(wd0, wd0), zero);
        f32x4 dk1 = mfma(selD, cat(wd1, wd1), zero);
        ac0 = mfma(selL, cat(wa0, wc0), ac0);
        ac1 = mfma(selL, cat(wa1, wc1), ac1);
        *(LDS_AS u32x4*)(minw) = u32x4{pk(dk0[0], dk0[1]), pk(dk0[2], dk0[3]), pk(ac0[0], ac0[1]), pk(ac0[2], ac0[3])};
        *(LDS_AS u32x4*)(minw + 512) = u32x4{pk(dk1[0], dk1[1]), pk(dk1[2], dk1[3]), pk(ac1[0], ac1[1]), pk(ac1[2], ac1[3])};
        if (wave < 4) {
            const float KS = -1.4426950408889634f;
            lptr xp = lds + T_OFF + c * 128;
            const u32x4 X0 = ld16(xp + ((g ^ (c & 6)) * 16)), X1 = ld16(xp + (((4 + g) ^ (c & 6)) * 16));
            const u32x2 b1v = ld8(sp8 + 128 + wave * 32);
            lptr wq = mini + 8192;
            f32x4 ab = {lo16(b1v[0]), hi16(b1v[0]), lo16(b1v[1]), hi16(b1v[1])};
            ab = mfma(cat(trread(wq), trread(wq + 512)), X0, ab);
            f32x4 ac = mfma(cat(wc0, wc1), X0, zero);
            ab = mfma(cat(trread(wq + 4096), trread(wq + 4096 + 512)), X1, ab);
            ac = mfma(cat(trread(wq + 8192 + 4096), trread(wq + 8192 + 4096 + 512)), X1, ac);
            *(LDS_AS f32x4*)(lds + B_OFF + c * BP + wave * 64 + g * 16) = (ab - ac) * KS;
        } else if (wave == 4) {
            unsigned v = *(LDS_AS unsigned short*)(lds + S_OFF + 256);
            *(LDS_AS unsigned*)(lds + S_OFF + 512 + (lane & 3) * 4) = v << 16;
        }
        A[0][0][0] = TA0[0] & m0; A[0][0][1] = TA0[1] & m1;
        A[0][1][0] = TA0[2] & m0; A[0][1][1] = TA0[3] & m1;
        A[0][2][0] = TA1[0] & m0; A[0][2][1] = TA1[1] & m1;
        A[0][3][0] = TA1[2] & m0; A[0][3][1] = TA1[3] & m1;
        A[1][0][0] = TB0[0] & m0; A[1][0][1] = TB0[1] & m1;
        A[1][1][0] = TB0[2] & m0; A[1][1][1] = TB0[3] & m1;
        A[1][2][0] = TB1[0] & m0; A[1][2][1] = TB1[1] & m1;
        A[1][3][0] = TB1[2] & m0; A[1][3][1] = TB1[3] & m1;
    }
    BARRIER();
    STAMP(5);

    u32x4 F[16];
#pragma unroll
    for (int k = 0; k < 16; ++k) F[k] = ld16(wb[k >> 2] + ((k >> 1) & 1) * 4096 + (k & 1) * 512);
    u32x4 W20 = cat(ld8(sp8), ld8(sp8 + 32));
    u32x4 W21 = cat(ld8(sp8 + 64), ld8(sp8 + 96));
    f32x4 b2c = ld16f(lds + S_OFF + 512);
    SB();
    if (__builtin_expect(cntA > 32, 0)) dma3(hbase + 4096, ld16(ip + 16) + choff, srd);
    if (__builtin_expect(cntB > 32, 0)) dma3(hbase + HREG + 4096, ld16(ip + 512 + 16) + choff, srd);
    dma4(hbase + HREG, ib, srd);
    SB();

    u32x4 WA[2][4][2];
    {
        f32x4 e[32];
#pragma unroll
        for (int k = 0; k < 34; ++k) {
            if (k < 32) e[k] = mfma(A[k >> 4][k & 3], F[k & 15], zero);
            if (k >= 2) {
                const int j = k - 2;
                unsigned x = pk(e[j][0], e[j][1]), y = pk(e[j][2], e[j][3]);
                PIN(x); PIN(y);
                WA[j >> 4][(j >> 2) & 3][(j >> 1) & 1][(j & 1) * 2] = x;
                WA[j >> 4][(j >> 2) & 3][(j >> 1) & 1][(j & 1) * 2 + 1] = y;
            }
            SB();
        }
    }
    STAMP(6);
    asm volatile("s_waitcnt vmcnt(4)" ::: "memory");
    STAMP(7);
    const float rA = run_batch<0>(a, lane, cntA, WA[0], W20, W21, b2c);
    STAMP(12);
    asm volatile("s_waitcnt vmcnt(0)" ::: "memory");
    STAMP(13);
    *(unsigned short*)(out + ooff) = (unsigned short)pk(rA, rA);
    const float rB = run_batch<1>(a, lane, cntB, WA[1], W20, W21, b2c);
    *(unsigned short*)(out + ooff + 128) = (unsigned short)pk(rB, rB);
}

extern "C" void kernel_launch(void* const* d_in, const int* in_sizes, int n_in,
                              void* d_out, int out_size, void* d_ws, size_t ws_size,
                              hipStream_t stream) {
    const int B = in_sizes[0] / 64;
    din_kernel<<<B / NB, NW * 64, 0, stream>>>((const char*)d_in[0], (const char*)d_in[1], (const char*)d_in[2],
                                               (const char*)d_in[3], (const char*)d_in[4], (const char*)d_in[5],
                                               (const char*)d_in[6], (char*)d_out
    );
}
```

```cpp
#include <hip/hip_runtime.h>

typedef __bf16 bf16x8 __attribute__((ext_vector_type(8)));
typedef __bf16 bf16x2 __attribute__((ext_vector_type(2)));
typedef short s16x4 __attribute__((ext_vector_type(4)));
typedef float f32x4 __attribute__((ext_vector_type(4)));
typedef float f32x2 __attribute__((ext_vector_type(2)));
typedef unsigned u32x4 __attribute__((ext_vector_type(4)));
typedef unsigned u32x2 __attribute__((ext_vector_type(2)));
#define LDS_AS __attribute__((address_space(3)))
#define DI static __device__ __forceinline__
typedef LDS_AS char* lptr;

constexpr int LH = 50;
constexpr int NW = 8;
constexpr int NB = 16;
constexpr int W_OFF = 0;
constexpr int T_OFF = 32768;
constexpr int S_OFF = T_OFF + 2048;
constexpr int BP = 272;
constexpr int B_OFF = S_OFF + 1024;
constexpr int M_OFF = B_OFF + NB * BP;
constexpr int H_OFF = M_OFF + NB * 512;
constexpr int HREG = 56 * 128;
constexpr int LDS_TOTAL = H_OFF + NB * HREG;
static_assert(LDS_TOTAL <= 163840 && H_OFF % 256 == 0 && M_OFF % 256 == 0, "lds");

DI unsigned pk(float a, float b) {
    f32x2 p = {a, b};
    return __builtin_bit_cast(unsigned, __builtin_convertvector(p, bf16x2));
}
DI f32x4 mfma(u32x4 a, u32x4 b, f32x4 c) {
    return __builtin_amdgcn_mfma_f32_16x16x32_bf16(__builtin_bit_cast(bf16x8, a), __builtin_bit_cast(bf16x8, b), c, 0, 0, 0);
}
DI lptr L(unsigned a) { return (lptr)(size_t)a; }
DI u32x2 trread(unsigned a) { return __builtin_bit_cast(u32x2, __builtin_amdgcn_ds_read_tr16_b64_v4i16((LDS_AS s16x4*)L(a))); }
DI u32x4 ld16(unsigned a) { return *(LDS_AS u32x4*)L(a); }
DI f32x4 ld16f(unsigned a) { return *(LDS_AS f32x4*)L(a); }
DI u32x2 ld8(unsigned a) { return *(LDS_AS u32x2*)L(a); }
DI unsigned ld4(unsigned a) { return *(LDS_AS unsigned*)L(a); }
DI void st4(unsigned a, unsigned v) { *(LDS_AS unsigned*)L(a) = v; }
DI void st16(unsigned a, u32x4 v) { *(LDS_AS u32x4*)L(a) = v; }
DI u32x4 cat(u32x2 a, u32x2 b) { return u32x4{a[0], a[1], b[0], b[1]}; }
DI float lo16(unsigned u) { return __uint_as_float(u << 16); }
DI float hi16(unsigned u) { return __uint_as_float(u & 0xffff0000u); }
DI float ex2(float x) { return __builtin_amdgcn_exp2f(x); }
DI float rcp(float x) { return __builtin_amdgcn_rcpf(x); }
#define PIN(x) asm volatile("" : "+v"(x))
#define SB() __builtin_amdgcn_sched_barrier(0)
#define BARRIER() do { asm volatile("s_waitcnt lgkmcnt(0)" ::: "memory"); __builtin_amdgcn_s_barrier(); asm volatile("" ::: "memory"); } while (0)

#define STAMP(i)
#define RSTAMP(i)

#define HNT ""

DI void dma4(unsigned ldsbase, u32x4 o, u32x4 srd, unsigned soff) {
    asm volatile("s_mov_b32 m0, %0\n\t"
                 "s_nop 0\n\t"
                 "buffer_load_dwordx4 %1, %5, %6 offen" HNT " lds\n\t"
                 "s_add_u32 m0, m0, 0x400\n\t"
                 "s_nop 0\n\t"
                 "buffer_load_dwordx4 %2, %5, %6 offen" HNT " lds\n\t"
                 "s_add_u32 m0, m0, 0x400\n\t"
                 "s_nop 0\n\t"
                 "buffer_load_dwordx4 %3, %5, %6 offen" HNT " lds\n\t"
                 "s_add_u32 m0, m0, 0x400\n\t"
                 "s_nop 0\n\t"
                 "buffer_load_dwordx4 %4, %5, %6 offen" HNT " lds"
                 :: "s"(ldsbase), "v"(o[0]), "v"(o[1]), "v"(o[2]), "v"(o[3]), "s"(srd), "s"(soff) : "memory", "scc");
}
DI void dma3(unsigned ldsbase, unsigned o0, unsigned o1, unsigned o2, u32x4 srd, unsigned soff) {
    asm volatile("s_mov_b32 m0, %0\n\t"
                 "s_nop 0\n\t"
                 "buffer_load_dwordx4 %1, %4, %5 offen" HNT " lds\n\t"
                 "s_add_u32 m0, m0, 0x400\n\t"
                 "s_nop 0\n\t"
                 "buffer_load_dwordx4 %2, %4, %5 offen" HNT " lds\n\t"
                 "s_add_u32 m0, m0, 0x400\n\t"
                 "s_nop 0\n\t"
                 "buffer_load_dwordx4 %3, %4, %5 offen" HNT " lds"
                 :: "s"(ldsbase), "v"(o0), "v"(o1), "v"(o2), "s"(srd), "s"(soff) : "memory", "scc");
}

DI float pick(f32x4 (&o)[4], unsigned lane) {
    PIN(o[0]); PIN(o[1]); PIN(o[2]); PIN(o[3]);
    const float o0 = o[0][0], o1 = o[1][0], o2 = o[2][0], o3 = o[3][0];
    const float r01 = (lane & 16) ? o1 : o0;
    const float r23 = (lane & 16) ? o3 : o2;
    return (lane & 32) ? r23 : r01;
}

DI float extra_tiles(unsigned hreg, unsigned marr, unsigned bp, unsigned lane, int cnt, const u32x4 (&WA)[4][2],
                     const u32x4& W20, const u32x4& W21, const f32x4& b2c) {
    const f32x4 zero = {0.f, 0.f, 0.f, 0.f};
    PIN(lane);
    const unsigned c = lane & 15, g = lane >> 4, trow = lane >> 2;
    float r = 0.f;
    for (int row = 32; row < cnt; row += 16) {
        const unsigned ro = row == 32 ? 32 : 40;
        const unsigned rr = hreg + (ro + c) * 128 + ((g ^ (c & 6)) * 16);
        const u32x4 h0 = ld16(rr), h1 = ld16(rr ^ 64);
        f32x4 mm = ld16f(marr + 256 + ro * 4 + g * 16);
        if (row != 32 && lane < 32) mm = zero;
        unsigned hp[8];
#pragma unroll
        for (int nt = 0; nt < 4; ++nt) {
            const f32x4 z = mfma(WA[nt][1], h1, mfma(WA[nt][0], h0, ld16f(bp + nt * 64)));
            f32x4 t = {ex2(z[0]), ex2(z[1]), ex2(z[2]), ex2(z[3])};
            t += 1.f;
            hp[2 * nt] = pk(rcp(t[0]), rcp(t[1]));
            hp[2 * nt + 1] = pk(rcp(t[2]), rcp(t[3]));
        }
        f32x4 sc = mfma(u32x4{hp[0], hp[1], hp[2], hp[3]}, W20, b2c);
        sc = mfma(u32x4{hp[4], hp[5], hp[6], hp[7]}, W21, sc) * mm;
        const u32x4 sa = {pk(sc[0], sc[1]), pk(sc[2], sc[3]), 0u, 0u};
        const unsigned tb = hreg + (ro + trow) * 128 + (trow & 6) * 16 + (lane & 3) * 8;
        f32x4 o[4];
#pragma unroll
        for (int dq = 0; dq < 4; ++dq) { const u32x2 x = trread(tb ^ (dq * 32)); o[dq] = mfma(sa, cat(x, x), zero); }
        r += pick(o, lane);
    }
    return r;
}

#define EXP2(v, i) do { v[i] = ex2(v[i]); v[(i) + 1] = ex2(v[(i) + 1]); } while (0)
#define EXP4(v) do { v[0] = ex2(v[0]); v[1] = ex2(v[1]); v[2] = ex2(v[2]); v[3] = ex2(v[3]); } while (0)
#define FIN4(v, x, y) do { f32x4 t_ = v + 1.f; x = pk(rcp(t_[0]), rcp(t_[1])); y = pk(rcp(t_[2]), rcp(t_[3])); } while (0)

__global__ __launch_bounds__(NW * 64) void din_kernel(const char* __restrict__ tgt, const char* __restrict__ hist,
                                                      const char* __restrict__ mask, const char* __restrict__ w1,
                                                      const char* __restrict__ b1, const char* __restrict__ w2,
                                                      const char* __restrict__ b2, char* __restrict__ out
) {
    __shared__ __attribute__((aligned(256))) char lds_[LDS_TOTAL];
    asm volatile("" :: "v"((lptr)lds_));
    const unsigned tid = threadIdx.x, lane = tid & 63;
    const unsigned wave = __builtin_amdgcn_readfirstlane(tid >> 6);
    const bool early = wave < 4;
    const unsigned b0 = blockIdx.x * NB;
    const unsigned b = b0 + wave * 2;
    unsigned l2 = lane * 2, l16 = lane * 16;

    __builtin_amdgcn_s_setprio(3);
    asm volatile("" :: "s"(tgt), "s"(hist), "s"(mask), "s"(w1), "s"(b1), "s"(w2), "s"(b2), "s"(out));
    unsigned mvA = 0, mvB = 0;
    {
        unsigned off = l2 < 2 * (LH - 1) ? l2 : 2 * (LH - 1);
        asm volatile("global_load_short_d16_hi %0, %2, %3\n\tglobal_load_short_d16_hi %1, %2, %3 offset:100"
                     : "+v"(mvA), "+v"(mvB) : "v"(off), "s"(mask + b * (LH * 2)));
    }
    const u32x4 wsrd = {(unsigned)(size_t)w1, (unsigned)((size_t)w1 >> 32) & 0xffffu, 32768u, 0x00020000u};
    unsigned wo = l16 ^ ((lane & 16) << 2) ^ ((wave & 1) << 5);
    asm volatile("s_mov_b32 m0, %0\n\ts_nop 0\n\t"
                 "buffer_load_dwordx4 %1, %2, m0 offen lds\n\t"
                 "s_add_u32 m0, m0, 0x4000\n\ts_nop 0\n\t"
                 "buffer_load_dwordx4 %1, %2, m0 offen lds\n\t"
                 "s_add_u32 m0, m0, 0x2000\n\ts_nop 0\n\t"
                 "buffer_load_dwordx4 %1, %2, m0 offen lds"
                 :: "s"(wave * 1024), "v"(wo), "s"(wsrd) : "memory", "scc");

    const unsigned marr = M_OFF + wave * 1024;
    const unsigned hreg = H_OFF + wave * (2 * HREG);
    {
        const unsigned a = marr + lane * 4;
        st4(a, 0x40000000u); st4(a + 256, 0u); st4(a + 512, 0x40000000u); st4(a + 768, 0u);
    }
    const unsigned p = lane & 3, q = (lane >> 2) & 3, g = lane >> 4;
    unsigned l128 = lane * 128;
    unsigned pos16 = l16 & 0x70;
    unsigned ip = (l2 & 0x70) | marr;
    unsigned g16 = lane & 48;
    const unsigned qp = q * 128 + p * 8 + ((q & 2) << 5);
    unsigned mini = (qp ^ (g * 32)) ^ (wave * 1024 + ((wave & 1) << 5));
    unsigned minw = l16 + wave * 1024;
    const unsigned long long diag = (q == g) ? 0xffffull << (p * 16) : 0ull;
    unsigned m0 = (unsigned)diag, m1 = (unsigned)(diag >> 32);
    PIN(l128); PIN(pos16); PIN(ip); PIN(mini); PIN(minw); PIN(m0); PIN(m1);
    const unsigned KH = 0xbfb9bfb9u, KL = 0x3b2c3b2cu, ONE = 0x3f803f80u;
    u32x4 selH = {m0 & KH, m1 & KH, m0 & KH, m1 & KH};
    u32x4 selL = {m0 & KL, m1 & KL, m0 & KL, m1 & KL};
    u32x4 selD = {m0 & KH, m1 & KH, m0 & KL, m1 & KL};
    PIN(selH); PIN(selL); PIN(selD);
    SB();
    asm volatile("s_mov_b32 m0, %0\n\ts_nop 0\n\tbuffer_load_dwordx4 %1, %2, m0 offen lds"
                 :: "s"(wave * 1024 + 8192), "v"(wo), "s"(wsrd) : "memory");
    if (wave >= 6) {
        unsigned off = l16 ^ (l2 & 96);
        asm volatile("s_mov_b32 m0, %0\n\ts_nop 0\n\tglobal_load_lds_dwordx4 %1, %2"
                     :: "s"(T_OFF + (wave & 1) * 1024), "v"(off), "s"(tgt + (b0 + (wave & 1) * 8) * 128) : "memory");
    } else if (wave == 5) {
        const char* pp = (lane < 32 ? w2 : b1) + (lane & 31) * 4;
        asm volatile("s_mov_b32 m0, %0\n\ts_nop 0\n\tglobal_load_lds_dword %1, off"
                     :: "s"(S_OFF), "v"(pp) : "memory");
    } else {
        unsigned off = 0;
        asm volatile("s_mov_b32 m0, %0\n\ts_nop 0\n\tglobal_load_lds_ushort %1, %2"
                     :: "s"(S_OFF + 256), "v"(off), "s"(b2) : "memory");
    }
    unsigned rr0 = hreg + p * 1024 + q * 128 + (g16 ^ (p * 32));
    unsigned rr1 = rr0 ^ 64;
    unsigned tb0 = hreg + q * (1024 + 32) + g * 128 + p * 8;
    unsigned tb1 = tb0 ^ 32, tb2 = tb0 ^ 64, tb3 = tb0 ^ 96;
    unsigned mp = g16 | marr;
    unsigned bp = g16 + (B_OFF + wave * (2 * BP));
    unsigned tp0 = g16 ^ (T_OFF + wave * 256 + (wave & 3) * 32);
    unsigned tp1 = tp0 ^ 64;
    unsigned fb = g * 1024 + (l16 & 255);
    unsigned sp8 = g * 8 + S_OFF;
    unsigned ooff = l2;
    PIN(rr0); PIN(rr1); PIN(tb0); PIN(tb1); PIN(tb2); PIN(tb3); PIN(mp); PIN(bp); PIN(tp0); PIN(tp1); PIN(fb); PIN(sp8);
    u32x4 A[4];
#pragma unroll
    for (int i = 0; i < 4; ++i) { A[i][2] = m0 & ONE; A[i][3] = m1 & ONE; PIN(A[i]); }
    u32x4 srd;
    {
        unsigned long long hb = (unsigned long long)hist + (unsigned long long)b * (LH * 128);
        srd[0] = (unsigned)hb;
        srd[1] = (unsigned)(hb >> 32) & 0xffffu;
        srd[2] = 2 * LH * 128;
        srd[3] = 0x00020000u;
    }
    asm volatile("" :: "s"(srd), "s"(hreg));

    asm volatile("s_waitcnt vmcnt(5)" : "+v"(mvA), "+v"(mvB) :: "memory");
    STAMP(2);

    unsigned long long MA, MB;
    asm volatile("v_cmp_neq_f32 %0, 0, %2\n\tv_cmp_neq_f32 %1, 0, %3\n\ts_nop 0" : "=&s"(MA), "=&s"(MB) : "v"(mvA), "v"(mvB));
    MA &= (1ull << LH) - 1;
    MB &= (1ull << LH) - 1;
    u32x4 ia, ib, wac0, wac1, wdd0, wdd1;
    {
        const unsigned ra = __builtin_amdgcn_mbcnt_hi((unsigned)(MA >> 32), __builtin_amdgcn_mbcnt_lo((unsigned)MA, 0)) * 4 + marr;
        const unsigned rb = __builtin_amdgcn_mbcnt_hi((unsigned)(MB >> 32), __builtin_amdgcn_mbcnt_lo((unsigned)MB, 0)) * 4 + marr;
        u32x2 a0, c0, d0, e0, a1, c1, d1, e1;
        asm volatile("s_mov_b64 exec, %10\n\t"
                     "ds_write2st64_b32 %12, %14, %15 offset1:1\n\t"
                     "s_mov_b64 exec, %11\n\t"
                     "ds_write2st64_b32 %13, %14, %16 offset0:2 offset1:3\n\t"
                     "s_mov_b64 exec, -1\n\t"
                     "ds_read_b128 %0, %17\n\t"
                     "ds_read_b128 %1, %17 offset:512\n\t"
                     "s_waitcnt vmcnt(2)\n\t"
                     "ds_read_b64_tr_b16 %2, %18\n\t"
                     "ds_read_b64_tr_b16 %3, %18 offset:16384\n\t"
                     "ds_read_b64_tr_b16 %6, %18 offset:512\n\t"
                     "ds_read_b64_tr_b16 %7, %18 offset:16896\n\t"
                     "ds_read_b64_tr_b16 %4, %18 offset:24576\n\t"
                     "ds_read_b64_tr_b16 %5, %18 offset:24576\n\t"
                     "ds_read_b64_tr_b16 %8, %18 offset:25088\n\t"
                     "ds_read_b64_tr_b16 %9, %18 offset:25088\n\t"
                     "s_waitcnt lgkmcnt(8)"
                     : "=&v"(ia), "=&v"(ib), "=&v"(a0), "=&v"(c0), "=&v"(d0), "=&v"(e0), "=&v"(a1), "=&v"(c1), "=&v"(d1), "=&v"(e1)
                     : "s"(MA), "s"(MB), "v"(ra), "v"(rb), "v"(l128), "v"(mvA), "v"(mvB), "v"(ip), "v"(mini) : "memory");
        wac0 = cat(a0, c0); wdd0 = cat(d0, e0); wac1 = cat(a1, c1); wdd1 = cat(d1, e1);
    }
    const int cntA = __builtin_popcount((unsigned)MA) + __builtin_popcount((unsigned)(MA >> 32));
    const int cntB = __builtin_popcount((unsigned)MB) + __builtin_popcount((unsigned)(MB >> 32));
    const bool big = cntA > 32 || cntB > 32;
    {
        const u32x4 sw = {pos16, pos16 ^ 32, pos16 ^ 64, pos16 ^ 96};
        ia |= sw; ib |= sw;
        PIN(ia); PIN(ib);
    }
    auto dma_rest = [&]() {
        if (__builtin_expect(big, 0)) {
            const unsigned ie = marr + 128 + (lane >> 3) * 4, ch = pos16 ^ (l2 & 96);
            dma3(hreg + 4096, ld4(ie) | ch, ld4(ie + 32) | ch, ld4(ie + 64) | ch, srd, 0);
            dma3(hreg + HREG + 4096, ld4(ie + 512) | ch, ld4(ie + 512 + 32) | ch, ld4(ie + 512 + 64) | ch, srd, LH * 128);
        }
        dma4(hreg + HREG, ib, srd, LH * 128);
    };
    if (early) dma4(hreg, ia, srd, 0);
    asm volatile("s_waitcnt lgkmcnt(0)" : "+v"(wac0), "+v"(wac1), "+v"(wdd0), "+v"(wdd1) :: "memory");
    SB();
    STAMP(3);

    const f32x4 zero = {0.f, 0.f, 0.f, 0.f};
    {
        f32x4 ac0 = mfma(selH, wac0, zero);
        f32x4 ac1 = mfma(selH, wac1, zero);
        f32x4 dk0 = mfma(selD, wdd0, zero);
        ac0 = mfma(selL, wac0, ac0);
        f32x4 dk1 = mfma(selD, wdd1, zero);
        ac1 = mfma(selL, wac1, ac1);
        SB();
        st16(minw, u32x4{pk(dk0[0], dk0[1]), pk(dk0[2], dk0[3]), pk(ac0[0], ac0[1]), pk(ac0[2], ac0[3])});
        st16(minw + 24576, u32x4{pk(dk1[0], dk1[1]), pk(dk1[2], dk1[3]), pk(ac1[0], ac1[1]), pk(ac1[2], ac1[3])});
    }
    if (early) {
        asm volatile("s_waitcnt vmcnt(4)" ::: "memory");
        dma_rest();
    } else {
        asm volatile("s_waitcnt vmcnt(0)" ::: "memory");
    }
    STAMP(4);
    BARRIER();
    STAMP(5);

    if (early) __builtin_amdgcn_s_setprio(2);
    if (!early) {
        const float KS = -1.4426950408889634f;
        const unsigned nt = wave & 3;
        const unsigned wq = (g * 1024 + qp) ^ ((g & 1) << 5) ^ (nt * 32);
        const unsigned x0 = T_OFF + (l128 & 0x780) + (g16 ^ (l16 & 0x60));
        const u32x4 X0 = ld16(x0), X1 = ld16(x0 ^ 64);
        const u32x4 wb0 = cat(trread(wq + 8192), trread(wq + 8192 + 512));
        const u32x4 wc0 = cat(trread(wq + 16384), trread(wq + 16384 + 512));
        const u32x4 wb1 = cat(trread(wq + 8192 + 4096), trread(wq + 8192 + 4096 + 512));
        const u32x4 wc1 = cat(trread(wq + 16384 + 4096), trread(wq + 16384 + 4096 + 512));
        const u32x2 b1v = ld8(sp8 + 128 + nt * 32);
        const unsigned b2v = *(LDS_AS unsigned short*)L(S_OFF + 256);
        SB();
        dma4(hreg, ia, srd, 0);
        SB();
        f32x4 ab = mfma(wb0, X0, zero);
        f32x4 ac = mfma(wc0, X0, zero);
        ab = mfma(wb1, X1, ab);
        ac = mfma(wc1, X1, ac);
        const f32x4 bb = {lo16(b1v[0]), hi16(b1v[0]), lo16(b1v[1]), hi16(b1v[1])};
        st4(S_OFF + 512 + lane * 4, b2v << 16);
        st16(B_OFF + (lane & 15) * BP + nt * 64 + g16, __builtin_bit_cast(u32x4, (ab + bb - ac) * KS));
        SB();
        dma_rest();
    }

    u32x4 F[16];
#define FREAD(k) F[k] = ld16(fb + ((k) >> 2) * 256 + (((k) >> 1) & 1) * 4096 + ((k) & 1) * 24576)
    const u32x4 TA0 = ld16(tp0);
    FREAD(0); FREAD(1);
    const u32x4 TA1 = ld16(tp1);
    FREAD(2); FREAD(3); FREAD(4); FREAD(5);
    SB();
    if (!early) BARRIER();
    SB();
    A[0][0] = TA0[0] & m0; A[0][1] = TA0[1] & m1;
    A[1][0] = TA0[2] & m0; A[1][1] = TA0[3] & m1;
    A[2][0] = TA1[0] & m0; A[2][1] = TA1[1] & m1;
    A[3][0] = TA1[2] & m0; A[3][1] = TA1[3] & m1;
    PIN(A[0]); PIN(A[1]); PIN(A[2]); PIN(A[3]);
    SB();
    u32x4 WA[2][4][2];
    u32x4 W20, W21, TB0, TB1, h00, h01, h10, h11;
    f32x4 b2c, biasA[4], biasB[4];
    {
        f32x4 e[32];
        constexpr int DIST = 3;
#pragma unroll
        for (int k = 0; k < 32 + DIST; ++k) {
            if (k < 32) e[k] = mfma(A[k & 3], F[k & 15], zero);
            SB();
            if (k < 10) FREAD(k + 6);
            if (k == 4) TB0 = ld16(tp0 + 128);
            if (k == 5) TB1 = ld16(tp1 + 128);
            if (k >= 12 && k < 16) {
                const u32x4& T = (k & 2) ? TB1 : TB0;
                A[k & 3][0] = T[(k & 1) * 2] & m0; A[k & 3][1] = T[(k & 1) * 2 + 1] & m1;
                PIN(A[k & 3]);
            }
            if (k >= DIST) {
                const int j = k - DIST;
                unsigned x = pk(e[j][0], e[j][1]), y = pk(e[j][2], e[j][3]);
                PIN(x); PIN(y);
                WA[j >> 4][(j >> 2) & 3][(j >> 1) & 1][(j & 1) * 2] = x;
                WA[j >> 4][(j >> 2) & 3][(j >> 1) & 1][(j & 1) * 2 + 1] = y;
            }
            if (k == 18) W20 = cat(ld8(sp8), ld8(sp8 + 32));
            if (k == 19) W21 = cat(ld8(sp8 + 64), ld8(sp8 + 96));
            if (k == 24) {
                STAMP(6);
                if (early) __builtin_amdgcn_s_barrier();
                asm volatile("s_waitcnt vmcnt(4)" ::: "memory");
                STAMP(7);
            }
            if (k == 24) { h00 = ld16(rr0); biasA[0] = ld16f(bp); }
            if (k == 25) { biasA[1] = ld16f(bp + 64); biasA[2] = ld16f(bp + 128); }
            if (k == 26) { biasA[3] = ld16f(bp + 192); h01 = ld16(rr1); }
            if (k == 27) { h10 = ld16(rr0 + 512); h11 = ld16(rr1 + 512); }
            if (k == 28) b2c = ld16f(S_OFF + 512);
            SB();
        }
    }
    float rA = 0.f, rB = 0.f;
    if (__builtin_expect(big, 0)) {
        rA = extra_tiles(hreg, marr, bp, lane, cntA, WA[0], W20, W21, b2c);
        rB = extra_tiles(hreg + HREG, marr + 512, bp + BP, lane, cntB, WA[1], W20, W21, b2c);
    }

    __builtin_amdgcn_s_setprio(2);
#define ADD1(v) do { v += 1.f; PIN(v); } while (0)
#define RCP4(v) do { v[0] = rcp(v[0]); v[1] = rcp(v[1]); v[2] = rcp(v[2]); v[3] = rcp(v[3]); PIN(v); } while (0)
#define CVT8(a, b) u32x4{pk(a[0], a[1]), pk(a[2], a[3]), pk(b[0], b[1]), pk(b[2], b[3])}
    f32x4 z0[4], z1[4], y0[4], y1[4];
    u32x4 hp0, hp1, sa, xa[4];
    f32x4 o[4];
#pragma unroll
    for (int nt = 0; nt < 4; ++nt) z0[nt] = mfma(WA[0][nt][0], h00, biasA[nt]);
#pragma unroll
    for (int nt = 0; nt < 4; ++nt) z0[nt] = mfma(WA[0][nt][1], h01, z0[nt]);
    SB();
    f32x4 mA0, mA1;
#pragma unroll
    for (int nt = 0; nt < 4; ++nt) {
        z1[nt] = mfma(WA[0][nt][0], h10, biasA[nt]); SB(); EXP2(z0[nt >> 1], (nt & 1) * 2); SB();
        biasB[nt] = ld16f(bp + BP + nt * 64); SB();
    }
    {
        const unsigned tb[4] = {tb0, tb1, tb2, tb3};
#pragma unroll
        for (int nt = 0; nt < 4; ++nt) {
            z1[nt] = mfma(WA[0][nt][1], h11, z1[nt]); SB(); EXP2(z0[2 + (nt >> 1)], (nt & 1) * 2); SB();
            xa[nt] = cat(trread(tb[nt]), trread(tb[nt] + 512)); SB();
        }
    }
    ADD1(z0[0]); ADD1(z0[1]); ADD1(z0[2]); ADD1(z0[3]);
    SB();
    RCP4(z0[0]); mA0 = ld16f(mp + 256); SB(); RCP4(z0[1]); mA1 = ld16f(mp + 256 + 64); SB(); RCP4(z0[2]); SB();
    hp0 = CVT8(z0[0], z0[1]); PIN(hp0);
    SB();
    RCP4(z0[3]);
    SB();
    f32x4 sA0 = mfma(hp0, W20, b2c);
    SB();
    EXP4(z1[0]);
    SB();
    hp1 = CVT8(z0[2], z0[3]); PIN(hp1);
    SB();
    EXP4(z1[1]);
    SB();
    sA0 = mfma(hp1, W21, sA0);
    SB();
    __builtin_amdgcn_s_setprio(1);
    asm volatile("s_waitcnt vmcnt(0)" ::: "memory");
    const u32x4 g00 = ld16(rr0 + HREG), g01 = ld16(rr1 + HREG);
    SB();
    EXP4(z1[2]);
    SB();
    const u32x4 g10 = ld16(rr0 + HREG + 512), g11 = ld16(rr1 + HREG + 512);
    SB();
    EXP4(z1[3]);
    SB();
    ADD1(z1[0]); ADD1(z1[1]);
    sA0 *= mA0; PIN(sA0);
    ADD1(z1[2]); ADD1(z1[3]);
    sa[0] = pk(sA0[0], sA0[1]); sa[1] = pk(sA0[2], sA0[3]); PIN(sa);
    SB();
#pragma unroll
    for (int nt = 0; nt < 4; ++nt) {
        y0[nt] = mfma(WA[1][nt][0], g00, biasB[nt]); SB();
        z1[nt >> 1][(nt & 1) * 2] = rcp(z1[nt >> 1][(nt & 1) * 2]); z1[nt >> 1][(nt & 1) * 2 + 1] = rcp(z1[nt >> 1][(nt & 1) * 2 + 1]); PIN(z1[nt >> 1]); SB();
    }
    y0[0] = mfma(WA[1][0][1], g01, y0[0]); SB();
    z1[2][0] = rcp(z1[2][0]); z1[2][1] = rcp(z1[2][1]); PIN(z1[2]); SB();
    y0[1] = mfma(WA[1][1][1], g01, y0[1]); SB();
    hp0 = CVT8(z1[0], z1[1]); PIN(hp0); SB();
    y0[2] = mfma(WA[1][2][1], g01, y0[2]); SB();
    z1[2][2] = rcp(z1[2][2]); z1[2][3] = rcp(z1[2][3]); PIN(z1[2]); SB();
    f32x4 sA1 = mfma(hp0, W20, b2c); SB();
    z1[3][0] = rcp(z1[3][0]); z1[3][1] = rcp(z1[3][1]); PIN(z1[3]); SB();
    y0[3] = mfma(WA[1][3][1], g01, y0[3]); SB();
    z1[3][2] = rcp(z1[3][2]); z1[3][3] = rcp(z1[3][3]); PIN(z1[3]); SB();
    y1[0] = mfma(WA[1][0][0], g10, biasB[0]); SB();
    EXP2(y0[0], 0); SB();
    hp1 = CVT8(z1[2], z1[3]); PIN(hp1); SB();
    y1[1] = mfma(WA[1][1][0], g10, biasB[1]); SB();
    EXP2(y0[0], 2); SB();
    sA1 = mfma(hp1, W21, sA1); SB();
    EXP2(y0[1], 0); SB();
    y1[2] = mfma(WA[1][2][0], g10, biasB[2]); SB();
    EXP2(y0[1], 2); SB();
    y1[3] = mfma(WA[1][3][0], g10, biasB[3]); SB();
    EXP2(y0[2], 0); SB();
    y1[0] = mfma(WA[1][0][1], g11, y1[0]); SB();
    sA1 *= mA1; PIN(sA1); SB();
    y1[1] = mfma(WA[1][1][1], g11, y1[1]); SB();
    EXP2(y0[2], 2); SB();
    sa[2] = pk(sA1[0], sA1[1]); sa[3] = pk(sA1[2], sA1[3]); PIN(sa); SB();
    y1[2] = mfma(WA[1][2][1], g11, y1[2]); SB();
    EXP2(y0[3], 0); SB();
    y1[3] = mfma(WA[1][3][1], g11, y1[3]); SB();
    EXP2(y0[3], 2); SB();
#pragma unroll
    for (int dq = 0; dq < 4; ++dq) { o[dq] = mfma(sa, xa[dq], zero); SB(); ADD1(y0[dq]); SB(); }
    f32x4 mB0, mB1;
    __builtin_amdgcn_s_setprio(0);
    RCP4(y0[0]); mB0 = ld16f(mp + 768); SB();
    RCP4(y0[1]); mB1 = ld16f(mp + 768 + 64); SB();
    rA += pick(o, lane);
    *(unsigned short*)(out + b * 128 + ooff) = (unsigned short)pk(rA, rA);
    SB();
    STAMP(12);
    STAMP(13);
    RCP4(y0[2]);
    xa[0] = cat(trread(tb0 + HREG), trread(tb0 + HREG + 512)); SB();
    hp0 = CVT8(y0[0], y0[1]); PIN(hp0);
    xa[1] = cat(trread(tb1 + HREG), trread(tb1 + HREG + 512)); SB();
    RCP4(y0[3]);
    xa[2] = cat(trread(tb2 + HREG), trread(tb2 + HREG + 512)); SB();
    f32x4 sB0 = mfma(hp0, W20, b2c);
    SB();
    EXP4(y1[0]);
    xa[3] = cat(trread(tb3 + HREG), trread(tb3 + HREG + 512)); SB();
    hp1 = CVT8(y0[2], y0[3]); PIN(hp1);
    SB();
    EXP4(y1[1]);
    SB();
    sB0 = mfma(hp1, W21, sB0);
    SB();
    EXP4(y1[2]); SB();
    ADD1(y1[0]); ADD1(y1[1]); SB();
    EXP4(y1[3]); SB();
    RCP4(y1[0]); SB();
    ADD1(y1[2]); ADD1(y1[3]); SB();
    RCP4(y1[1]); SB();
    sB0 *= mB0; PIN(sB0); SB();
    RCP4(y1[2]); SB();
    hp0 = CVT8(y1[0], y1[1]); PIN(hp0); SB();
    RCP4(y1[3]); SB();
    f32x4 sB1 = mfma(hp0, W20, b2c); SB();
    hp1 = CVT8(y1[2], y1[3]); PIN(hp1); SB();
    sa[0] = pk(sB0[0], sB0[1]); sa[1] = pk(sB0[2], sB0[3]); PIN(sa); SB();
    sB1 = mfma(hp1, W21, sB1);
    sB1 *= mB1;
    sa[2] = pk(sB1[0], sB1[1]); sa[3] = pk(sB1[2], sB1[3]);
#pragma unroll
    for (int dq = 0; dq < 4; ++dq) o[dq] = mfma(sa, xa[dq], zero);
    rB += pick(o, lane);
    *(unsigned short*)(out + b * 128 + 128 + ooff) = (unsigned short)pk(rB, rB);
}

extern "C" void kernel_launch(void* const* d_in, const int* in_sizes, int n_in,
                              void* d_out, int out_size, void* d_ws, size_t ws_size,
                              hipStream_t stream) {
    const int B = in_sizes[0] / 64;
    din_kernel<<<B / NB, NW * 64, 0, stream>>>((const char*)d_in[0], (const char*)d_in[1], (const char*)d_in[2],
                                               (const char*)d_in[3], (const char*)d_in[4], (const char*)d_in[5],
                                               (const char*)d_in[6], (char*)d_out
    );
}
```

```cpp
#include <hip/hip_runtime.h>

constexpr int NW = 8;
constexpr int NB = 16;
constexpr int LDS_TOTAL = 159232;

__global__ __launch_bounds__(NW * 64) void din_kernel(const void* w1, const void* hist, const void* b2, void* out,
                                                      const void* tgt, const void* mask, const void* b1, const void* w2) {
    __shared__ __attribute__((aligned(256))) char lds_[LDS_TOTAL];
    asm volatile("" :: "v"((__attribute__((address_space(3))) char*)lds_));
    asm volatile(
        "s_load_dwordx8 s[16:23], s[0:1], 0x20\n\t"
        "s_load_dwordx2 s[48:49], s[0:1], 0x28\n\t"
        "s_load_dwordx2 s[8:9], s[0:1], 0x0\n\t"
        "s_load_dwordx4 s[12:15], s[0:1], 0x10\n\t"
        "s_load_dwordx2 s[40:41], s[0:1], 0x8\n\t"
        "s_getpc_b64 s[76:77]\n\t"
        ".Ldin_pc:\n\t"
        "v_lshlrev_b32 v30, 7, v0\n\t"
        "v_cmp_gt_u32 vcc, .Ldin_last-.Ldin_pc, v30\n\t"
        "s_nop 4\n\t"
        "s_cbranch_vccz .Ldin_npf\n\t"
        "v_min_u32 v30, .Ldin_last-.Ldin_pc, v30\n\t"
        "global_load_dword v255, v30, s[76:77]\n\t"
        ".Ldin_npf:\n\t"
        "v_readfirstlane_b32 s28, v0\n\t"
        "v_and_b32 v1, 63, v0\n\t"
        "s_setprio 3\n\t"
        "s_lshr_b32 s28, s28, 6\n\t"
        "v_lshlrev_b32 v64, 1, v1\n\t"
        "s_lshl_b32 s29, s2, 4\n\t"
        "v_lshlrev_b32 v29, 2, v1\n\t"
        "s_lshl1_add_u32 s29, s28, s29\n\t"
        "v_lshlrev_b32 v65, 4, v1\n\t"
        "s_mul_i32 s4, s29, 100\n\t"
        "s_lshl_b32 s5, s29, 7\n\t"
        "v_add_u32 v68, s4, v64\n\t"
        "v_cmp_gt_u32 vcc, 50, v1\n\t"
        "s_mov_b32 s50, 0x3fffffff\n\t"
        "s_mov_b32 s51, 0x20000\n\t"
        "v_cndmask_b32 v68, 2.0, v68, vcc\n\t"
        "v_add_u32 v2, s5, v64\n\t"
        "v_add_u32 v70, s5, v29\n\t"
        "s_and_b32 s4, s28, 1\n\t"
        "v_bitop3_b32 v67, v29, v65, 64 bitop3:0x6c\n\t"
        "s_lshl_b32 s4, s4, 5\n\t"
        "v_mov_b32 v80, 0\n\t"
        "v_mov_b32 v81, 0\n\t"
        "v_xor_b32 v67, s4, v67\n\t"
        "s_lshl_b32 s5, s28, 10\n\t"
        "s_or_b32 s4, s4, s5\n\t"
        "s_add_u32 s30, s5, 48640\n\t"
        "s_mul_i32 s31, s28, 12800\n\t"
        "s_add_u32 s31, s31, 56832\n\t"
        "v_mov_b32 v92, 0\n\t"
        "v_lshlrev_b32 v66, 7, v1\n\t"
        "v_and_b32 v79, 0x70, v64\n\t"
        "v_or_b32 v69, s30, v79\n\t"
        "v_and_b32 v76, 0x70, v65\n\t"
        "v_bitop3_b32 v122, v1, v76, 16 bitop3:0x6c\n\t"
        "v_add_u32 v74, s5, v65\n\t"
        "v_lshlrev_b32 v76, 5, v1\n\t"
        "v_and_b32 v77, 0x180, v76\n\t"
        "v_lshlrev_b32 v78, 3, v1\n\t"
        "v_and_or_b32 v77, v78, 64, v77\n\t"
        "v_and_or_b32 v75, v65, 16, v77\n\t"
        "v_and_or_b32 v75, v29, 8, v75\n\t"
        "v_and_or_b32 v24, v78, 24, v77\n\t"
        "v_and_b32 v79, 0x60, v64\n\t"
        "v_bitop3_b32 v73, v75, v79, s4 bitop3:0x96\n\t"
        "v_mov_b32 v120, 2.0\n\t"
        "v_or_b32 v72, s30, v29\n\t"
        "s_mov_b32 s10, 32768\n\t"
        "s_mov_b32 s11, 0x20000\n\t"
        "v_mov_b32 v31, 0xffff\n\t"
        "s_mov_b32 s46, 0xffff0000\n\t"
        "s_mov_b32 s47, 0xffff0000\n\t"
        "v_lshlrev_b32 v31, v65, v31\n\t"
        "s_mov_b32 s76, 3\n\t"
        "s_mov_b32 s77, 48\n\t"
        "v_cndmask_b32 v216, 0, v31, s[76:77]\n\t"
        "s_lshl_b64 s[76:77], s[76:77], 2\n\t"
        "v_cndmask_b32 v217, 0, v31, s[76:77]\n\t"
        "s_lshl_b64 s[76:77], s[76:77], 6\n\t"
        "v_cndmask_b32 v218, 0, v31, s[76:77]\n\t"
        "s_lshl_b64 s[76:77], s[76:77], 2\n\t"
        "v_cndmask_b32 v219, 0, v31, s[76:77]\n\t"
        "s_lshl_b64 s[76:77], s[76:77], 6\n\t"
        "v_cndmask_b32 v220, 0, v31, s[76:77]\n\t"
        "s_lshl_b64 s[76:77], s[76:77], 2\n\t"
        "v_cndmask_b32 v221, 0, v31, s[76:77]\n\t"
        "s_lshl_b64 s[76:77], s[76:77], 6\n\t"
        "v_cndmask_b32 v222, 0, v31, s[76:77]\n\t"
        "s_lshl_b64 s[76:77], s[76:77], 2\n\t"
        "v_cndmask_b32 v223, 0, v31, s[76:77]\n\t"
        "v_and_b32 v31, 0x3f803f80, v31\n\t"
        "s_mov_b32 s76, 0x00300003\n\t"
        "s_mov_b32 s77, 0x30000300\n\t"
        "v_cndmask_b32 v96, 0, v31, s[76:77]\n\t"
        "v_cndmask_b32 v98, 0, v31, s[76:77]\n\t"
        "s_lshl_b64 s[76:77], s[76:77], 2\n\t"
        "v_cndmask_b32 v97, 0, v31, s[76:77]\n\t"
        "v_cndmask_b32 v99, 0, v31, s[76:77]\n\t"
        "s_mov_b32 s42, 0x3fffffff\n\t"
        "s_mov_b32 s43, 0x20000\n\t"
        "s_mov_b32 s44, 0xbfb8aa3b\n\t"
        "s_mov_b32 s45, 0xbfb8aa3b\n\t"
        "s_movk_i32 s72, 3072\n\t"
        "s_movk_i32 s73, 2080\n\t"
        "s_movk_i32 s74, 1088\n\t"
        "s_movk_i32 s75, 96\n\t"
        "ds_write2st64_b32 v72, v120, v92 offset1:1\n\t"
        "ds_write2st64_b32 v72, v120, v92 offset0:2 offset1:3\n\t"
        "s_mul_i32 s6, s29, 6400\n\t"
        "s_add_u32 s7, s6, 6400\n\t"
        "s_add_u32 s3, s31, 6400\n\t"
        "s_and_b32 s78, s28, 3\n\t"
        "s_lshl_b32 s78, s78, 5\n\t"
        "s_mul_i32 s79, s28, 288\n\t"
        "v_and_b32 v77, 32, v1\n\t"
        "v_lshrrev_b32 v76, 1, v77\n\t"
        "v_add3_u32 v121, v76, v29, s79\n\t"
        "s_add_u32 s76, s31, 5120\n\t"
        "v_add_u32 v124, s76, v29\n\t"
        "v_lshlrev_b32 v30, 5, v1\n\t"
        "v_and_b32 v30, 0x1e0, v30\n\t"
        "v_lshrrev_b32 v31, 2, v1\n\t"
        "v_and_or_b32 v30, v31, 12, v30\n\t"
        "s_add_u32 s76, s31, 4096\n\t"
        "v_add_u32 v123, s76, v30\n\t"
        "v_and_b32 v30, 0x130, v65\n\t"
        "v_and_or_b32 v30, v78, 64, v30\n\t"
        "v_lshl_or_b32 v30, v77, 2, v30\n\t"
        "v_add_u32 v71, s76, v30\n\t"
        "v_bitop3_b32 v9, v64, v75, 32 bitop3:0x6c\n\t"
        "v_xor_b32 v9, v77, v9\n\t"
        "v_lshl_or_b32 v9, v77, 5, v9\n\t"
        "v_xor_b32 v10, 64, v9\n\t"
        "v_lshl_add_u32 v8, v77, 4, v65\n\t"
        "v_and_b32 v30, 0x198, v78\n\t"
        "v_xor_b32 v30, v30, v76\n\t"
        "v_and_b32 v31, 12, v1\n\t"
        "v_mul_u32_u24 v31, 264, v31\n\t"
        "v_add3_u32 v16, v30, v31, s31\n\t"
        "v_xor_b32 v17, 32, v16\n\t"
        "v_xor_b32 v18, 64, v16\n\t"
        "v_xor_b32 v19, 96, v16\n\t"
        "s_mul_i32 s76, s28, 544\n\t"
        "v_add_u32 v11, s76, v77\n\t"
        "v_and_b32 v207, 48, v1\n\t"
        "v_and_or_b32 v20, v78, 64, v207\n\t"
        "v_or_b32 v20, s30, v20\n\t"
        "v_mov_b32 v3, 0\n\t"
        "s_mov_b32 s76, 0xff0000ff\n\t"
        "s_mov_b32 s77, 0xff0000ff\n\t"
        "v_mov_b32 v31, 768\n\t"
        "v_cndmask_b32 v21, v31, v76, s[76:77]\n\t"
        "v_and_b32 v30, 3, v1\n\t"
        "v_mul_u32_u24 v208, 1056, v30\n\t"
        "v_bitop3_b32 v31, v64, v76, 16 bitop3:0x6c\n\t"
        "v_and_b32 v207, 28, v1\n\t"
        "v_lshl_or_b32 v31, v207, 5, v31\n\t"
        "v_add3_u32 v12, v208, v31, s31\n\t"
        "v_xor_b32 v13, 32, v12\n\t"
        "v_xor_b32 v14, 64, v12\n\t"
        "v_xor_b32 v15, 96, v12\n\t"
        "s_cmp_lt_u32 s28, 4\n\t"
        "s_cbranch_scc1 .Ldin_y\n\t"
        "v_and_b32 v79, 48, v1\n\t"
        "v_lshl_or_b32 v24, v79, 6, v24\n\t"
        "v_bitop3_b32 v24, v64, v24, 32 bitop3:0x6c\n\t"
        "v_xor_b32 v24, s78, v24\n\t"
        "v_lshrrev_b32 v27, 1, v79\n\t"
        "v_add_u32 v27, s78, v27\n\t"
        "v_and_b32 v31, 15, v1\n\t"
        "v_mul_u32_u24 v30, 144, v31\n\t"
        "v_add_u32 v25, v30, v79\n\t"
        "v_mul_u32_u24 v30, 272, v31\n\t"
        "v_lshl_add_u32 v28, v27, 1, v30\n\t"
        ".Ldin_y:\n\t"
        "s_xor_b32 s52, s28, 4\n\t"
        "s_lshl_b32 s79, s52, 7\n\t"
        "s_add_u32 m0, s79, 43264\n\t"
        "s_cmp_gt_u32 s52, 2\n\t"
        "s_waitcnt lgkmcnt(0)\n\t"
        "s_cbranch_scc1 .Ldin_x3\n\t"
        "s_cmp_eq_u32 s52, 2\n\t"
        "s_cbranch_scc1 .Ldin_x2\n\t"
        "s_cmp_eq_u32 s52, 0\n\t"
        "s_cselect_b64 s[76:77], s[22:23], s[20:21]\n\t"
        "s_mov_b32 exec_hi, 0\n\t"
        "global_load_lds_dword v29, s[76:77]\n\t"
        "s_mov_b32 exec_hi, -1\n\t"
        "s_branch .Ldin_x3\n\t"
        ".Ldin_x2:\n\t"
        "global_load_lds_ushort v92, s[12:13]\n\t"
        "ds_write_b32 v29, v92 offset:44032\n\t"
        ".Ldin_x3:\n\t"
        "s_mov_b32 m0, s5\n\t"
        "s_nop 0\n\t"
        "buffer_load_dwordx4 v67, s[8:11], m0 offen lds\n\t"
        "s_add_u32 m0, m0, 16384\n\t"
        "s_nop 0\n\t"
        "buffer_load_dwordx4 v67, s[8:11], m0 offen lds\n\t"
        "s_add_u32 m0, m0, 8192\n\t"
        "buffer_load_short_d16_hi v80, v68, s[48:51], 0 offen\n\t"
        "buffer_load_short_d16_hi v81, v68, s[48:51], 0 offen offset:100\n\t"
        "global_load_dword v108, v70, s[16:17]\n\t"
        "buffer_load_dwordx4 v67, s[8:11], m0 offen lds\n\t"
        "s_sub_u32 m0, m0, 16384\n\t"
        "s_nop 0\n\t"
        "buffer_load_dwordx4 v67, s[8:11], m0 offen lds\n\t"
        "s_mov_b32 m0, s31\n\t"
        "s_sub_u32 s40, s40, 3072\n\t"
        "s_subb_u32 s41, s41, 0\n\t"
        "s_waitcnt vmcnt(5)\n\t"
        "ds_read_b64_tr_b16 v[100:101], v73\n\t"
        "ds_read_b64_tr_b16 v[102:103], v73 offset:16384\n\t"
        "ds_read_b64_tr_b16 v[104:105], v73 offset:512\n\t"
        "ds_read_b64_tr_b16 v[106:107], v73 offset:16896\n\t"
        "s_waitcnt lgkmcnt(2)\n\t"
        "v_mfma_f32_16x16x32_bf16 v[112:115], v[96:99], v[100:103], 0\n\t"
        "s_waitcnt lgkmcnt(0)\n\t"
        "v_mfma_f32_16x16x32_bf16 v[116:119], v[96:99], v[104:107], 0\n\t"
        "s_nop 5\n\t"
        "v_pk_mul_f32 v[112:113], v[112:113], s[44:45]\n\t"
        "v_pk_mul_f32 v[114:115], v[114:115], s[44:45]\n\t"
        "v_pk_mul_f32 v[116:117], v[116:117], s[44:45]\n\t"
        "v_pk_mul_f32 v[118:119], v[118:119], s[44:45]\n\t"
        "ds_write_b128 v74, v[112:115]\n\t"
        "ds_write_b128 v74, v[116:119] offset:32768\n\t"
        "s_waitcnt vmcnt(2)\n\t"
        "v_cmp_neq_f32 s[24:25], 0, v80\n\t"
        "v_cmp_neq_f32 s[26:27], 0, v81\n\t"
        "v_lshlrev_b32 v82, 16, v108\n\t"
        "v_and_b32 v83, 0xffff0000, v108\n\t"
        "v_mbcnt_lo_u32_b32 v76, s24, 0\n\t"
        "v_mbcnt_lo_u32_b32 v77, s26, 0\n\t"
        "v_pk_mul_f32 v[82:83], v[82:83], s[44:45]\n\t"
        "v_mbcnt_hi_u32_b32 v76, s25, v76\n\t"
        "v_mbcnt_hi_u32_b32 v77, s27, v77\n\t"
        "v_cvt_pk_bf16_f32 v78, v82, v82\n\t"
        "v_lshl_add_u32 v76, v76, 2, s30\n\t"
        "v_lshl_add_u32 v77, v77, 2, s30\n\t"
        "v_cvt_pk_bf16_f32 v79, v83, v83\n\t"
        "v_cndmask_b32 v76, v124, v76, s[24:25]\n\t"
        "v_cndmask_b32 v77, v124, v77, s[26:27]\n\t"
        "v_cvt_pk_bf16_f32 v82, v82, v83\n\t"
        "ds_write2st64_b32 v76, v66, v80 offset1:1\n\t"
        "ds_read_b128 v[192:195], v69\n\t"
        "ds_write2_b32 v123, v78, v79 offset0:0 offset1:4\n\t"
        "ds_read_b128 v[212:215], v71\n\t"
        "ds_write2st64_b32 v77, v66, v81 offset0:2 offset1:3\n\t"
        "ds_read_b128 v[4:7], v69 offset:512\n\t"
        "ds_write_b32 v121, v82 offset:40960\n\t"
        "s_bcnt1_i32_b64 s37, s[24:25]\n\t"
        "s_bcnt1_i32_b64 s38, s[26:27]\n\t"
        "s_max_u32 s39, s37, s38\n\t"
        "s_cmp_gt_u32 s39, 32\n\t"
        "s_cselect_b64 vcc, -1, 0\n\t"
        "s_cmp_lt_u32 s28, 4\n\t"
        "s_waitcnt lgkmcnt(5)\n\t"
        "v_xad_u32 v192, v122, s72, v192\n\t"
        "v_xad_u32 v193, v122, s73, v193\n\t"
        "v_xad_u32 v194, v122, s74, v194\n\t"
        "v_xad_u32 v195, v122, s75, v195\n\t"
        "s_cbranch_scc0 .Ldin_a1\n\t"
        "buffer_load_dwordx4 v192, s[40:43], s6 offen lds\n\t"
        "buffer_load_dwordx4 v193, s[40:43], s6 offen offset:1024 lds\n\t"
        "buffer_load_dwordx4 v194, s[40:43], s6 offen offset:2048 lds\n\t"
        "buffer_load_dwordx4 v195, s[40:43], s6 offen offset:3072 lds\n\t"
        "s_or_b32 m0, s3, s3\n\t"
        ".Ldin_a1:\n\t"
        "s_waitcnt lgkmcnt(1)\n\t"
        "v_xad_u32 v4, v122, s72, v4\n\t"
        "v_xad_u32 v5, v122, s73, v5\n\t"
        "v_xad_u32 v6, v122, s74, v6\n\t"
        "v_xad_u32 v7, v122, s75, v7\n\t"
        "v_mov_b32 v90, 0\n\t"
        "s_cbranch_scc1 1\n\t"
        "s_waitcnt vmcnt(0)\n\t"
        "s_waitcnt vmcnt(4)\n\t"
        "s_waitcnt lgkmcnt(0)\n\t"
        "s_barrier\n\t"
        "s_cbranch_scc1 .Ldin_b1\n\t"
        "ds_read_b64 v[88:89], v27 offset:43392\n\t"
        "ds_read_b128 v[64:67], v25 offset:40960\n\t"
        "ds_read_b64_tr_b16 v[76:77], v24 offset:16384\n\t"
        "ds_read_b64_tr_b16 v[78:79], v24 offset:16896\n\t"
        "ds_read_b64_tr_b16 v[72:73], v24 offset:8192\n\t"
        "ds_read_b64_tr_b16 v[74:75], v24 offset:8704\n\t"
        "ds_read_b128 v[68:71], v25 offset:41024\n\t"
        "ds_read_b64_tr_b16 v[84:85], v24 offset:20480\n\t"
        "ds_read_b64_tr_b16 v[86:87], v24 offset:20992\n\t"
        "ds_read_b64_tr_b16 v[80:81], v24 offset:12288\n\t"
        "ds_read_b64_tr_b16 v[82:83], v24 offset:12800\n\t"
        "ds_read_u16_d16_hi v90, v90 offset:43520\n\t"
        "s_waitcnt lgkmcnt(11)\n\t"
        "v_lshlrev_b32 v92, 16, v88\n\t"
        "v_and_b32 v93, 0xffff0000, v88\n\t"
        "v_lshlrev_b32 v94, 16, v89\n\t"
        "v_and_b32 v95, 0xffff0000, v89\n\t"
        "v_pk_mul_f32 v[92:93], v[92:93], s[44:45]\n\t"
        "v_pk_mul_f32 v[94:95], v[94:95], s[44:45]\n\t"
        "s_waitcnt lgkmcnt(8)\n\t"
        "v_mfma_f32_16x16x32_bf16 v[96:99], v[76:79], v[64:67], 0\n\t"
        "s_waitcnt lgkmcnt(6)\n\t"
        "v_mfma_f32_16x16x32_bf16 v[92:95], v[72:75], v[64:67], v[92:95]\n\t"
        "s_waitcnt lgkmcnt(3)\n\t"
        "v_mfma_f32_16x16x32_bf16 v[96:99], v[84:87], v[68:71], v[96:99]\n\t"
        "s_waitcnt lgkmcnt(1)\n\t"
        "v_mfma_f32_16x16x32_bf16 v[92:95], v[80:83], v[68:71], v[92:95]\n\t"
        "s_waitcnt lgkmcnt(0)\n\t"
        "ds_write_b32 v29, v90 offset:43776\n\t"
        "s_nop 5\n\t"
        "v_pk_add_f32 v[92:93], v[92:93], v[96:97] neg_lo:[0,1] neg_hi:[0,1]\n\t"
        "v_pk_add_f32 v[94:95], v[94:95], v[98:99] neg_lo:[0,1] neg_hi:[0,1]\n\t"
        "ds_write_b128 v28, v[92:95] offset:44288\n\t"
        "buffer_load_dwordx4 v192, s[40:43], s6 offen lds\n\t"
        "buffer_load_dwordx4 v193, s[40:43], s6 offen offset:1024 lds\n\t"
        "buffer_load_dwordx4 v194, s[40:43], s6 offen offset:2048 lds\n\t"
        "buffer_load_dwordx4 v195, s[40:43], s6 offen offset:3072 lds\n\t"
        "s_mov_b32 m0, s3\n\t"
        "s_setprio 2\n\t"
        ".Ldin_b1:\n\t"
        "ds_read_b64_tr_b16 v[176:177], v9 offset:24576\n\t"
        "ds_read_b64_tr_b16 v[178:179], v9 offset:25088\n\t"
        "ds_read_b128 v[128:131], v8\n\t"
        "ds_read_b128 v[132:135], v8 offset:32768\n\t"
        "ds_read_b128 v[136:139], v8 offset:2048\n\t"
        "ds_read_b128 v[140:143], v8 offset:34816\n\t"
        "ds_read_b64_tr_b16 v[180:181], v9 offset:26624\n\t"
        "ds_read_b64_tr_b16 v[182:183], v9 offset:27136\n\t"
        "s_setprio 2\n\t"
        "v_and_b32 v32, v212, v216\n\t"
        "v_and_b32 v33, v212, v217\n\t"
        "v_and_b32 v34, v212, v218\n\t"
        "v_and_b32 v35, v212, v219\n\t"
        "v_and_b32 v36, v212, v220\n\t"
        "v_and_b32 v37, v212, v221\n\t"
        "v_and_b32 v38, v212, v222\n\t"
        "v_and_b32 v39, v212, v223\n\t"
        "s_cbranch_scc1 .Ldin_b2\n\t"
        "s_waitcnt lgkmcnt(8)\n\t"
        "s_barrier\n\t"
        ".Ldin_b2:\n\t"
        "s_waitcnt lgkmcnt(2)\n\t"
        "v_mfma_f32_32x32x16_bf16 v[192:207], v[32:35], v[176:179], v[128:143]\n\t"
        "ds_read_b64_tr_b16 v[184:185], v9 offset:28672\n\t"
        "ds_read_b64_tr_b16 v[186:187], v9 offset:29184\n\t"
        "ds_read_b128 v[144:147], v8 offset:4096\n\t"
        "ds_read_b128 v[148:151], v8 offset:36864\n\t"
        "ds_read_b128 v[152:155], v8 offset:6144\n\t"
        "ds_read_b128 v[156:159], v8 offset:38912\n\t"
        "ds_read_b64_tr_b16 v[188:189], v9 offset:30720\n\t"
        "ds_read_b64_tr_b16 v[190:191], v9 offset:31232\n\t"
        "v_and_b32 v40, v213, v216\n\t"
        "v_and_b32 v41, v213, v217\n\t"
        "v_and_b32 v42, v213, v218\n\t"
        "v_and_b32 v43, v213, v219\n\t"
        "v_and_b32 v44, v213, v220\n\t"
        "v_and_b32 v45, v213, v221\n\t"
        "v_and_b32 v46, v213, v222\n\t"
        "v_and_b32 v47, v213, v223\n\t"
        "s_waitcnt lgkmcnt(8)\n\t"
        "v_mfma_f32_32x32x16_bf16 v[192:207], v[36:39], v[180:183], v[192:207]\n\t"
        "v_and_b32 v48, v214, v216\n\t"
        "v_and_b32 v49, v214, v217\n\t"
        "v_and_b32 v50, v214, v218\n\t"
        "v_and_b32 v51, v214, v219\n\t"
        "v_and_b32 v52, v214, v220\n\t"
        "v_and_b32 v53, v214, v221\n\t"
        "v_and_b32 v54, v214, v222\n\t"
        "v_and_b32 v55, v214, v223\n\t"
        "v_mfma_f32_32x32x16_bf16 v[128:143], v[48:51], v[176:179], v[128:143]\n\t"
        "v_and_b32 v56, v215, v216\n\t"
        "v_and_b32 v57, v215, v217\n\t"
        "v_and_b32 v58, v215, v218\n\t"
        "v_and_b32 v59, v215, v219\n\t"
        "v_and_b32 v60, v215, v220\n\t"
        "v_and_b32 v61, v215, v221\n\t"
        "v_and_b32 v62, v215, v222\n\t"
        "v_and_b32 v63, v215, v223\n\t"
        "s_waitcnt lgkmcnt(2)\n\t"
        "v_mfma_f32_32x32x16_bf16 v[208:223], v[40:43], v[184:187], v[144:159]\n\t"
        "buffer_load_dwordx4 v4, s[40:43], s7 offen lds\n\t"
        "v_cvt_pk_bf16_f32 v64, v192, v193\n\t"
        "v_cvt_pk_bf16_f32 v65, v194, v195\n\t"
        "v_cvt_pk_bf16_f32 v66, v196, v197\n\t"
        "v_cvt_pk_bf16_f32 v67, v198, v199\n\t"
        "s_waitcnt lgkmcnt(0)\n\t"
        "v_mfma_f32_32x32x16_bf16 v[208:223], v[44:47], v[188:191], v[208:223]\n\t"
        "v_cvt_pk_bf16_f32 v68, v200, v201\n\t"
        "v_cvt_pk_bf16_f32 v69, v202, v203\n\t"
        "v_cvt_pk_bf16_f32 v70, v204, v205\n\t"
        "v_cvt_pk_bf16_f32 v71, v206, v207\n\t"
        "s_cbranch_scc0 .Ldin_b3\n\t"
        "s_barrier\n\t"
        ".Ldin_b3:\n\t"
        "s_waitcnt vmcnt(1)\n\t"
        "v_mfma_f32_32x32x16_bf16 v[128:143], v[52:55], v[180:183], v[128:143]\n\t"
        "ds_read_b128 v[224:227], v11 offset:44288\n\t"
        "ds_read_b128 v[228:231], v11 offset:44304\n\t"
        "ds_read_b128 v[232:235], v11 offset:44352\n\t"
        "ds_read_b128 v[236:239], v11 offset:44368\n\t"
        "ds_read_b128 v[240:243], v12\n\t"
        "ds_read_b128 v[244:247], v13\n\t"
        "ds_read_b128 v[248:251], v14\n\t"
        "ds_read_b128 v[252:255], v15\n\t"
        "v_mfma_f32_32x32x16_bf16 v[144:159], v[56:59], v[184:187], v[144:159]\n\t"
        "buffer_load_dwordx4 v5, s[40:43], s7 offen offset:1024 lds\n\t"
        "v_cvt_pk_bf16_f32 v72, v208, v209\n\t"
        "v_cvt_pk_bf16_f32 v73, v210, v211\n\t"
        "v_cvt_pk_bf16_f32 v74, v212, v213\n\t"
        "v_cvt_pk_bf16_f32 v75, v214, v215\n\t"
        "v_mfma_f32_32x32x16_bf16 v[144:159], v[60:63], v[188:191], v[144:159]\n\t"
        "v_cvt_pk_bf16_f32 v76, v216, v217\n\t"
        "v_cvt_pk_bf16_f32 v77, v218, v219\n\t"
        "v_cvt_pk_bf16_f32 v78, v220, v221\n\t"
        "v_cvt_pk_bf16_f32 v79, v222, v223\n\t"
        "s_waitcnt lgkmcnt(3)\n\t"
        "v_mfma_f32_32x32x16_bf16 v[224:239], v[64:67], v[240:243], v[224:239]\n\t"
        "v_cvt_pk_bf16_f32 v96, v128, v129\n\t"
        "v_cvt_pk_bf16_f32 v97, v130, v131\n\t"
        "v_cvt_pk_bf16_f32 v98, v132, v133\n\t"
        "v_cvt_pk_bf16_f32 v99, v134, v135\n\t"
        "s_waitcnt lgkmcnt(2)\n\t"
        "v_mfma_f32_32x32x16_bf16 v[224:239], v[68:71], v[244:247], v[224:239]\n\t"
        "buffer_load_dwordx4 v6, s[40:43], s7 offen offset:2048 lds\n\t"
        "v_cvt_pk_bf16_f32 v100, v136, v137\n\t"
        "v_cvt_pk_bf16_f32 v101, v138, v139\n\t"
        "v_cvt_pk_bf16_f32 v102, v140, v141\n\t"
        "v_cvt_pk_bf16_f32 v103, v142, v143\n\t"
        "s_waitcnt lgkmcnt(1)\n\t"
        "v_mfma_f32_32x32x16_bf16 v[224:239], v[72:75], v[248:251], v[224:239]\n\t"
        "v_cvt_pk_bf16_f32 v104, v144, v145\n\t"
        "v_cvt_pk_bf16_f32 v105, v146, v147\n\t"
        "v_cvt_pk_bf16_f32 v106, v148, v149\n\t"
        "v_cvt_pk_bf16_f32 v107, v150, v151\n\t"
        "s_waitcnt lgkmcnt(0)\n\t"
        "v_mfma_f32_32x32x16_bf16 v[224:239], v[76:79], v[252:255], v[224:239]\n\t"
        "ds_read_b64_tr_b16 v[176:177], v10 offset:24576\n\t"
        "ds_read_b64_tr_b16 v[178:179], v10 offset:25088\n\t"
        "ds_read_b128 v[160:163], v8 offset:512\n\t"
        "ds_read_b128 v[164:167], v8 offset:33280\n\t"
        "ds_read_b128 v[168:171], v8 offset:2560\n\t"
        "ds_read_b128 v[172:175], v8 offset:35328\n\t"
        "ds_read_b64_tr_b16 v[180:181], v10 offset:26624\n\t"
        "ds_read_b64_tr_b16 v[182:183], v10 offset:27136\n\t"
        "ds_read_b64_tr_b16 v[184:185], v10 offset:28672\n\t"
        "ds_read_b64_tr_b16 v[186:187], v10 offset:29184\n\t"
        "ds_read_b128 v[128:131], v8 offset:4608\n\t"
        "ds_read_b128 v[132:135], v8 offset:37376\n\t"
        "ds_read_b128 v[136:139], v8 offset:6656\n\t"
        "ds_read_b128 v[140:143], v8 offset:39424\n\t"
        "v_cvt_pk_bf16_f32 v108, v152, v153\n\t"
        "v_cvt_pk_bf16_f32 v109, v154, v155\n\t"
        "v_cvt_pk_bf16_f32 v110, v156, v157\n\t"
        "v_cvt_pk_bf16_f32 v111, v158, v159\n\t"
        "s_setprio 2\n\t"
        "v_exp_f32 v224, v224\n\t"
        "v_exp_f32 v225, v225\n\t"
        "v_exp_f32 v226, v226\n\t"
        "v_exp_f32 v227, v227\n\t"
        "ds_read_b64_tr_b16 v[188:189], v10 offset:30720\n\t"
        "ds_read_b64_tr_b16 v[190:191], v10 offset:31232\n\t"
        "ds_read_b128 v[144:147], v11 offset:44416\n\t"
        "ds_read_b128 v[148:151], v11 offset:44432\n\t"
        "ds_read_b128 v[152:155], v11 offset:44480\n\t"
        "ds_read_b128 v[156:159], v11 offset:44496\n\t"
        "ds_read_b128 v[24:27], v3 offset:43776\n\t"
        "buffer_load_dwordx4 v7, s[40:43], s7 offen offset:3072 lds\n\t"
        "s_cbranch_vccnz .Ldin_xdma\n\t"
        ".Ldin_xdma_ret:\n\t"
        "v_exp_f32 v228, v228\n\t"
        "v_exp_f32 v229, v229\n\t"
        "v_exp_f32 v230, v230\n\t"
        "v_exp_f32 v231, v231\n\t"
        "v_exp_f32 v232, v232\n\t"
        "v_exp_f32 v233, v233\n\t"
        "v_pk_add_f32 v[224:225], v[224:225], 1.0 op_sel_hi:[1,0]\n\t"
        "v_pk_add_f32 v[226:227], v[226:227], 1.0 op_sel_hi:[1,0]\n\t"
        "v_pk_add_f32 v[228:229], v[228:229], 1.0 op_sel_hi:[1,0]\n\t"
        "v_pk_add_f32 v[230:231], v[230:231], 1.0 op_sel_hi:[1,0]\n\t"
        "s_waitcnt lgkmcnt(15)\n\t"
        "v_mfma_f32_32x32x16_bf16 v[192:207], v[32:35], v[176:179], v[160:175]\n\t"
        "v_exp_f32 v234, v234\n\t"
        "v_exp_f32 v235, v235\n\t"
        "v_exp_f32 v236, v236\n\t"
        "s_waitcnt lgkmcnt(13)\n\t"
        "v_mfma_f32_32x32x16_bf16 v[192:207], v[36:39], v[180:183], v[192:207]\n\t"
        "v_exp_f32 v237, v237\n\t"
        "v_exp_f32 v238, v238\n\t"
        "v_exp_f32 v239, v239\n\t"
        "v_rcp_f32 v224, v224\n\t"
        "v_rcp_f32 v225, v225\n\t"
        "v_pk_add_f32 v[232:233], v[232:233], 1.0 op_sel_hi:[1,0]\n\t"
        "v_pk_add_f32 v[234:235], v[234:235], 1.0 op_sel_hi:[1,0]\n\t"
        "v_pk_add_f32 v[236:237], v[236:237], 1.0 op_sel_hi:[1,0]\n\t"
        "v_pk_add_f32 v[238:239], v[238:239], 1.0 op_sel_hi:[1,0]\n\t"
        "s_waitcnt lgkmcnt(7)\n\t"
        "v_mfma_f32_32x32x16_bf16 v[208:223], v[40:43], v[184:187], v[128:143]\n\t"
        "v_rcp_f32 v226, v226\n\t"
        "v_rcp_f32 v227, v227\n\t"
        "v_rcp_f32 v228, v228\n\t"
        "s_waitcnt lgkmcnt(5)\n\t"
        "v_mfma_f32_32x32x16_bf16 v[208:223], v[44:47], v[188:191], v[208:223]\n\t"
        "v_rcp_f32 v229, v229\n\t"
        "v_rcp_f32 v230, v230\n\t"
        "v_rcp_f32 v231, v231\n\t"
        "v_mfma_f32_32x32x16_bf16 v[160:175], v[48:51], v[176:179], v[160:175]\n\t"
        "v_cvt_pk_bf16_f32 v80, v192, v193\n\t"
        "v_cvt_pk_bf16_f32 v81, v194, v195\n\t"
        "v_cvt_pk_bf16_f32 v82, v196, v197\n\t"
        "v_cvt_pk_bf16_f32 v83, v198, v199\n\t"
        "v_cvt_pk_bf16_f32 v84, v200, v201\n\t"
        "v_cvt_pk_bf16_f32 v85, v202, v203\n\t"
        "v_cvt_pk_bf16_f32 v86, v204, v205\n\t"
        "v_cvt_pk_bf16_f32 v87, v206, v207\n\t"
        "v_rcp_f32 v232, v232\n\t"
        "v_rcp_f32 v233, v233\n\t"
        "v_cvt_pk_bf16_f32 v224, v224, v225\n\t"
        "v_cvt_pk_bf16_f32 v225, v226, v227\n\t"
        "v_mfma_f32_32x32x16_bf16 v[160:175], v[52:55], v[180:183], v[160:175]\n\t"
        "v_cvt_pk_bf16_f32 v226, v228, v229\n\t"
        "v_cvt_pk_bf16_f32 v227, v230, v231\n\t"
        "v_rcp_f32 v234, v234\n\t"
        "v_rcp_f32 v235, v235\n\t"
        "s_waitcnt lgkmcnt(1)\n\t"
        "v_mfma_f32_32x32x16_bf16 v[144:159], v[80:83], v[240:243], v[144:159]\n\t"
        "v_cvt_pk_bf16_f32 v88, v208, v209\n\t"
        "v_cvt_pk_bf16_f32 v89, v210, v211\n\t"
        "v_cvt_pk_bf16_f32 v90, v212, v213\n\t"
        "v_cvt_pk_bf16_f32 v91, v214, v215\n\t"
        "v_rcp_f32 v236, v236\n\t"
        "v_rcp_f32 v237, v237\n\t"
        "v_mfma_f32_32x32x16_bf16 v[144:159], v[84:87], v[244:247], v[144:159]\n\t"
        "v_cvt_pk_bf16_f32 v92, v216, v217\n\t"
        "v_cvt_pk_bf16_f32 v93, v218, v219\n\t"
        "v_cvt_pk_bf16_f32 v94, v220, v221\n\t"
        "v_cvt_pk_bf16_f32 v95, v222, v223\n\t"
        "v_rcp_f32 v238, v238\n\t"
        "v_rcp_f32 v239, v239\n\t"
        "v_mfma_f32_32x32x16_bf16 v[144:159], v[88:91], v[248:251], v[144:159]\n\t"
        "v_cvt_pk_bf16_f32 v112, v160, v161\n\t"
        "v_cvt_pk_bf16_f32 v113, v162, v163\n\t"
        "v_cvt_pk_bf16_f32 v114, v164, v165\n\t"
        "v_cvt_pk_bf16_f32 v115, v166, v167\n\t"
        "v_cvt_pk_bf16_f32 v232, v232, v233\n\t"
        "v_cvt_pk_bf16_f32 v233, v234, v235\n\t"
        "v_mfma_f32_32x32x16_bf16 v[144:159], v[92:95], v[252:255], v[144:159]\n\t"
        "ds_read_b128 v[240:243], v21 offset:43264\n\t"
        "ds_read_b128 v[244:247], v21 offset:43296\n\t"
        "ds_read_b128 v[248:251], v21 offset:43328\n\t"
        "ds_read_b128 v[252:255], v21 offset:43360\n\t"
        "ds_read_b128 v[192:195], v11 offset:44560\n\t"
        "ds_read_b128 v[196:199], v11 offset:44576\n\t"
        "ds_read_b128 v[200:203], v11 offset:44624\n\t"
        "ds_read_b128 v[204:207], v11 offset:44640\n\t"
        "v_cvt_pk_bf16_f32 v116, v168, v169\n\t"
        "v_cvt_pk_bf16_f32 v117, v170, v171\n\t"
        "v_cvt_pk_bf16_f32 v118, v172, v173\n\t"
        "v_cvt_pk_bf16_f32 v119, v174, v175\n\t"
        "v_cvt_pk_bf16_f32 v234, v236, v237\n\t"
        "v_cvt_pk_bf16_f32 v235, v238, v239\n\t"
        "v_mfma_f32_32x32x16_bf16 v[128:143], v[56:59], v[184:187], v[128:143]\n\t"
        "s_setprio 1\n\t"
        "v_exp_f32 v144, v144\n\t"
        "v_exp_f32 v145, v145\n\t"
        "v_exp_f32 v146, v146\n\t"
        "v_mfma_f32_32x32x16_bf16 v[128:143], v[60:63], v[188:191], v[128:143]\n\t"
        "s_waitcnt vmcnt(0)\n\t"
        "ds_read_b128 v[32:35], v12 offset:6400\n\t"
        "ds_read_b128 v[36:39], v13 offset:6400\n\t"
        "ds_read_b128 v[40:43], v14 offset:6400\n\t"
        "ds_read_b128 v[44:47], v15 offset:6400\n\t"
        "ds_read_b64_tr_b16 v[160:161], v16\n\t"
        "ds_read_b64_tr_b16 v[162:163], v16 offset:512\n\t"
        "ds_read_b64_tr_b16 v[164:165], v17\n\t"
        "ds_read_b64_tr_b16 v[166:167], v17 offset:512\n\t"
        "ds_read_b64_tr_b16 v[168:169], v18\n\t"
        "ds_read_b64_tr_b16 v[170:171], v18 offset:512\n\t"
        "ds_read_b64_tr_b16 v[172:173], v19\n\t"
        "ds_read_b64_tr_b16 v[174:175], v19 offset:512\n\t"
        "v_exp_f32 v147, v147\n\t"
        "v_exp_f32 v148, v148\n\t"
        "v_exp_f32 v149, v149\n\t"
        "v_exp_f32 v150, v150\n\t"
        "v_exp_f32 v151, v151\n\t"
        "v_exp_f32 v152, v152\n\t"
        "v_exp_f32 v153, v153\n\t"
        "v_pk_add_f32 v[144:145], v[144:145], 1.0 op_sel_hi:[1,0]\n\t"
        "v_pk_add_f32 v[146:147], v[146:147], 1.0 op_sel_hi:[1,0]\n\t"
        "v_pk_add_f32 v[148:149], v[148:149], 1.0 op_sel_hi:[1,0]\n\t"
        "v_pk_add_f32 v[150:151], v[150:151], 1.0 op_sel_hi:[1,0]\n\t"
        "s_waitcnt lgkmcnt(15)\n\t"
        "v_mfma_f32_16x16x32_bf16 v[4:7], v[224:227], v[240:243], v[24:27]\n\t"
        "ds_read_b128 v[208:211], v11 offset:44688\n\t"
        "ds_read_b128 v[212:215], v11 offset:44704\n\t"
        "ds_read_b128 v[216:219], v11 offset:44752\n\t"
        "ds_read_b128 v[220:223], v11 offset:44768\n\t"
        "ds_read_b64_tr_b16 v[48:49], v16 offset:6400\n\t"
        "ds_read_b64_tr_b16 v[50:51], v16 offset:6912\n\t"
        "ds_read_b64_tr_b16 v[52:53], v17 offset:6400\n\t"
        "ds_read_b64_tr_b16 v[54:55], v17 offset:6912\n\t"
        "ds_read_b64_tr_b16 v[56:57], v18 offset:6400\n\t"
        "ds_read_b64_tr_b16 v[58:59], v18 offset:6912\n\t"
        "ds_read_b64_tr_b16 v[60:61], v19 offset:6400\n\t"
        "ds_read_b64_tr_b16 v[62:63], v19 offset:6912\n\t"
        "ds_read_b128 v[176:179], v20 offset:256\n\t"
        "ds_read_b128 v[180:183], v20 offset:768\n\t"
        "v_exp_f32 v154, v154\n\t"
        "v_exp_f32 v155, v155\n\t"
        "v_mfma_f32_16x16x32_bf16 v[4:7], v[232:235], v[244:247], v[4:7]\n\t"
        "v_cvt_pk_bf16_f32 v120, v128, v129\n\t"
        "v_cvt_pk_bf16_f32 v121, v130, v131\n\t"
        "v_cvt_pk_bf16_f32 v122, v132, v133\n\t"
        "v_cvt_pk_bf16_f32 v123, v134, v135\n\t"
        "v_cvt_pk_bf16_f32 v124, v136, v137\n\t"
        "v_cvt_pk_bf16_f32 v125, v138, v139\n\t"
        "v_cvt_pk_bf16_f32 v126, v140, v141\n\t"
        "v_cvt_pk_bf16_f32 v127, v142, v143\n\t"
        "v_exp_f32 v156, v156\n\t"
        "v_exp_f32 v157, v157\n\t"
        "v_exp_f32 v158, v158\n\t"
        "v_exp_f32 v159, v159\n\t"
        "v_rcp_f32 v144, v144\n\t"
        "v_rcp_f32 v145, v145\n\t"
        "v_pk_add_f32 v[152:153], v[152:153], 1.0 op_sel_hi:[1,0]\n\t"
        "v_pk_add_f32 v[154:155], v[154:155], 1.0 op_sel_hi:[1,0]\n\t"
        "v_pk_add_f32 v[156:157], v[156:157], 1.0 op_sel_hi:[1,0]\n\t"
        "v_pk_add_f32 v[158:159], v[158:159], 1.0 op_sel_hi:[1,0]\n\t"
        "s_waitcnt lgkmcnt(15)\n\t"
        "v_mfma_f32_32x32x16_bf16 v[192:207], v[96:99], v[32:35], v[192:207]\n\t"
        "v_rcp_f32 v146, v146\n\t"
        "v_rcp_f32 v147, v147\n\t"
        "v_rcp_f32 v148, v148\n\t"
        "v_mfma_f32_32x32x16_bf16 v[192:207], v[100:103], v[36:39], v[192:207]\n\t"
        "v_rcp_f32 v149, v149\n\t"
        "v_rcp_f32 v150, v150\n\t"
        "v_rcp_f32 v151, v151\n\t"
        "v_mfma_f32_32x32x16_bf16 v[192:207], v[104:107], v[40:43], v[192:207]\n\t"
        "v_rcp_f32 v152, v152\n\t"
        "v_rcp_f32 v153, v153\n\t"
        "v_cvt_pk_bf16_f32 v144, v144, v145\n\t"
        "v_mfma_f32_32x32x16_bf16 v[192:207], v[108:111], v[44:47], v[192:207]\n\t"
        "v_cvt_pk_bf16_f32 v145, v146, v147\n\t"
        "v_cvt_pk_bf16_f32 v146, v148, v149\n\t"
        "v_cvt_pk_bf16_f32 v147, v150, v151\n\t"
        "v_rcp_f32 v154, v154\n\t"
        "v_rcp_f32 v155, v155\n\t"
        "v_mfma_f32_16x16x32_bf16 v[4:7], v[144:147], v[248:251], v[4:7]\n\t"
        "v_rcp_f32 v156, v156\n\t"
        "s_waitcnt lgkmcnt(10)\n\t"
        "v_mfma_f32_32x32x16_bf16 v[208:223], v[112:115], v[32:35], v[208:223]\n\t"
        "v_rcp_f32 v157, v157\n\t"
        "v_rcp_f32 v158, v158\n\t"
        "v_rcp_f32 v159, v159\n\t"
        "v_exp_f32 v192, v192\n\t"
        "v_cvt_pk_bf16_f32 v152, v152, v153\n\t"
        "v_cvt_pk_bf16_f32 v153, v154, v155\n\t"
        "v_cvt_pk_bf16_f32 v154, v156, v157\n\t"
        "v_cvt_pk_bf16_f32 v155, v158, v159\n\t"
        "v_exp_f32 v193, v193\n\t"
        "v_exp_f32 v194, v194\n\t"
        "v_mfma_f32_16x16x32_bf16 v[4:7], v[152:155], v[252:255], v[4:7]\n\t"
        "v_exp_f32 v195, v195\n\t"
        "v_exp_f32 v196, v196\n\t"
        "s_waitcnt lgkmcnt(0)\n\t"
        "v_mfma_f32_32x32x16_bf16 v[208:223], v[116:119], v[36:39], v[208:223]\n\t"
        "v_exp_f32 v197, v197\n\t"
        "v_exp_f32 v198, v198\n\t"
        "v_exp_f32 v199, v199\n\t"
        "v_exp_f32 v200, v200\n\t"
        "v_exp_f32 v201, v201\n\t"
        "v_pk_add_f32 v[192:193], v[192:193], 1.0 op_sel_hi:[1,0]\n\t"
        "v_pk_mul_f32 v[4:5], v[4:5], v[176:177]\n\t"
        "v_pk_mul_f32 v[6:7], v[6:7], v[178:179]\n\t"
        "v_pk_add_f32 v[194:195], v[194:195], 1.0 op_sel_hi:[1,0]\n\t"
        "v_pk_add_f32 v[196:197], v[196:197], 1.0 op_sel_hi:[1,0]\n\t"
        "v_cvt_pk_bf16_f32 v132, v4, v5\n\t"
        "v_cvt_pk_bf16_f32 v133, v6, v7\n\t"
        "v_pk_add_f32 v[198:199], v[198:199], 1.0 op_sel_hi:[1,0]\n\t"
        "v_mfma_f32_32x32x16_bf16 v[208:223], v[120:123], v[40:43], v[208:223]\n\t"
        "v_exp_f32 v202, v202\n\t"
        "v_exp_f32 v203, v203\n\t"
        "v_mov_b32_dpp v134, v132 row_ror:8 row_mask:0xf bank_mask:0xf\n\t"
        "v_mov_b32_dpp v135, v133 row_ror:8 row_mask:0xf bank_mask:0xf\n\t"
        "v_exp_f32 v204, v204\n\t"
        "v_mfma_f32_32x32x16_bf16 v[208:223], v[124:127], v[44:47], v[208:223]\n\t"
        "v_exp_f32 v205, v205\n\t"
        "v_exp_f32 v206, v206\n\t"
        "v_exp_f32 v207, v207\n\t"
        "v_rcp_f32 v192, v192\n\t"
        "v_rcp_f32 v193, v193\n\t"
        "v_pk_add_f32 v[200:201], v[200:201], 1.0 op_sel_hi:[1,0]\n\t"
        "v_pk_add_f32 v[202:203], v[202:203], 1.0 op_sel_hi:[1,0]\n\t"
        "v_pk_add_f32 v[204:205], v[204:205], 1.0 op_sel_hi:[1,0]\n\t"
        "v_pk_add_f32 v[206:207], v[206:207], 1.0 op_sel_hi:[1,0]\n\t"
        "v_mfma_f32_16x16x32_bf16 v[224:227], v[132:135], v[160:163], 0\n\t"
        "v_rcp_f32 v194, v194\n\t"
        "v_rcp_f32 v195, v195\n\t"
        "v_mfma_f32_16x16x32_bf16 v[228:231], v[132:135], v[164:167], 0\n\t"
        "v_rcp_f32 v196, v196\n\t"
        "v_rcp_f32 v197, v197\n\t"
        "v_mfma_f32_16x16x32_bf16 v[232:235], v[132:135], v[168:171], 0\n\t"
        "v_rcp_f32 v198, v198\n\t"
        "v_rcp_f32 v199, v199\n\t"
        "v_mfma_f32_16x16x32_bf16 v[236:239], v[132:135], v[172:175], 0\n\t"
        "v_rcp_f32 v200, v200\n\t"
        "v_rcp_f32 v201, v201\n\t"
        "v_cvt_pk_bf16_f32 v192, v192, v193\n\t"
        "v_cvt_pk_bf16_f32 v193, v194, v195\n\t"
        "v_cvt_pk_bf16_f32 v194, v196, v197\n\t"
        "v_cvt_pk_bf16_f32 v195, v198, v199\n\t"
        "v_rcp_f32 v202, v202\n\t"
        "v_rcp_f32 v203, v203\n\t"
        "v_mfma_f32_16x16x32_bf16 v[128:131], v[192:195], v[240:243], v[24:27]\n\t"
        "v_cndmask_b32 v22, v224, v228, s[46:47]\n\t"
        "v_rcp_f32 v204, v204\n\t"
        "v_rcp_f32 v205, v205\n\t"
        "v_cndmask_b32 v30, v232, v236, s[46:47]\n\t"
        "v_rcp_f32 v206, v206\n\t"
        "v_rcp_f32 v207, v207\n\t"
        "v_exp_f32 v208, v208\n\t"
        "v_permlane32_swap_b32 v22, v30\n\t"
        "v_cvt_pk_bf16_f32 v200, v200, v201\n\t"
        "v_cvt_pk_bf16_f32 v201, v202, v203\n\t"
        "v_cvt_pk_bf16_f32 v31, v22, v22\n\t"
        "global_store_short v2, v31, s[14:15]\n\t"
        "v_cvt_pk_bf16_f32 v202, v204, v205\n\t"
        "v_cvt_pk_bf16_f32 v203, v206, v207\n\t"
        "v_exp_f32 v209, v209\n\t"
        "v_exp_f32 v210, v210\n\t"
        "v_mfma_f32_16x16x32_bf16 v[128:131], v[200:203], v[244:247], v[128:131]\n\t"
        "s_cbranch_vccnz .Ldin_big\n\t"
        "s_setprio 0\n\t"
        "v_exp_f32 v211, v211\n\t"
        "v_exp_f32 v212, v212\n\t"
        "v_exp_f32 v213, v213\n\t"
        "v_exp_f32 v214, v214\n\t"
        "v_exp_f32 v215, v215\n\t"
        "v_exp_f32 v216, v216\n\t"
        "v_exp_f32 v217, v217\n\t"
        "v_pk_add_f32 v[208:209], v[208:209], 1.0 op_sel_hi:[1,0]\n\t"
        "v_pk_add_f32 v[210:211], v[210:211], 1.0 op_sel_hi:[1,0]\n\t"
        "v_pk_add_f32 v[212:213], v[212:213], 1.0 op_sel_hi:[1,0]\n\t"
        "v_pk_add_f32 v[214:215], v[214:215], 1.0 op_sel_hi:[1,0]\n\t"
        "v_exp_f32 v218, v218\n\t"
        "v_exp_f32 v219, v219\n\t"
        "v_exp_f32 v220, v220\n\t"
        "v_exp_f32 v221, v221\n\t"
        "v_exp_f32 v222, v222\n\t"
        "v_exp_f32 v223, v223\n\t"
        "v_rcp_f32 v208, v208\n\t"
        "v_rcp_f32 v209, v209\n\t"
        "v_pk_add_f32 v[216:217], v[216:217], 1.0 op_sel_hi:[1,0]\n\t"
        "v_pk_add_f32 v[218:219], v[218:219], 1.0 op_sel_hi:[1,0]\n\t"
        "v_pk_add_f32 v[220:221], v[220:221], 1.0 op_sel_hi:[1,0]\n\t"
        "v_pk_add_f32 v[222:223], v[222:223], 1.0 op_sel_hi:[1,0]\n\t"
        "v_rcp_f32 v210, v210\n\t"
        "v_rcp_f32 v211, v211\n\t"
        "v_rcp_f32 v212, v212\n\t"
        "v_rcp_f32 v213, v213\n\t"
        "v_rcp_f32 v214, v214\n\t"
        "v_rcp_f32 v215, v215\n\t"
        "v_rcp_f32 v216, v216\n\t"
        "v_rcp_f32 v217, v217\n\t"
        "v_cvt_pk_bf16_f32 v208, v208, v209\n\t"
        "v_cvt_pk_bf16_f32 v209, v210, v211\n\t"
        "v_cvt_pk_bf16_f32 v210, v212, v213\n\t"
        "v_cvt_pk_bf16_f32 v211, v214, v215\n\t"
        "v_rcp_f32 v218, v218\n\t"
        "v_rcp_f32 v219, v219\n\t"
        "v_mfma_f32_16x16x32_bf16 v[128:131], v[208:211], v[248:251], v[128:131]\n\t"
        "v_rcp_f32 v220, v220\n\t"
        "v_rcp_f32 v221, v221\n\t"
        "v_rcp_f32 v222, v222\n\t"
        "v_rcp_f32 v223, v223\n\t"
        "v_cvt_pk_bf16_f32 v216, v216, v217\n\t"
        "v_cvt_pk_bf16_f32 v217, v218, v219\n\t"
        "v_cvt_pk_bf16_f32 v218, v220, v221\n\t"
        "v_cvt_pk_bf16_f32 v219, v222, v223\n\t"
        "s_nop 1\n\t"
        "v_mfma_f32_16x16x32_bf16 v[128:131], v[216:219], v[252:255], v[128:131]\n\t"
        "s_nop 7\n\t"
        "v_pk_mul_f32 v[128:129], v[128:129], v[180:181]\n\t"
        "v_pk_mul_f32 v[130:131], v[130:131], v[182:183]\n\t"
        "v_cvt_pk_bf16_f32 v132, v128, v129\n\t"
        "v_cvt_pk_bf16_f32 v133, v130, v131\n\t"
        "s_nop 0\n\t"
        "v_mov_b32_dpp v134, v132 row_ror:8 row_mask:0xf bank_mask:0xf\n\t"
        "v_mov_b32_dpp v135, v133 row_ror:8 row_mask:0xf bank_mask:0xf\n\t"
        "s_nop 1\n\t"
        "v_mfma_f32_16x16x32_bf16 v[192:195], v[132:135], v[48:51], 0\n\t"
        "v_mfma_f32_16x16x32_bf16 v[196:199], v[132:135], v[52:55], 0\n\t"
        "v_mfma_f32_16x16x32_bf16 v[200:203], v[132:135], v[56:59], 0\n\t"
        "v_mfma_f32_16x16x32_bf16 v[204:207], v[132:135], v[60:63], 0\n\t"
        "s_nop 5\n\t"
        "v_cndmask_b32 v23, v192, v196, s[46:47]\n\t"
        "s_nop 0\n\t"
        "v_cndmask_b32 v30, v200, v204, s[46:47]\n\t"
        "s_nop 1\n\t"
        "v_permlane32_swap_b32 v23, v30\n\t"
        "v_cvt_pk_bf16_f32 v31, v23, v23\n\t"
        "global_store_short v2, v31, s[14:15] offset:128\n\t"
        ".Ldin_skip:\n\t"
        ".Ldin_end:\n\t"
        "s_endpgm\n\t"
        ".Ldin_xdma:\n\t"
        "v_lshrrev_b32 v30, 3, v1\n\t"
        "v_lshl_add_u32 v30, v30, 2, s30\n\t"
        "ds_read_b32 v4, v30 offset:128\n\t"
        "ds_read_b32 v5, v30 offset:160\n\t"
        "ds_read_b32 v6, v30 offset:192\n\t"
        "ds_read_b32 v7, v30 offset:640\n\t"
        "ds_read_b32 v8, v30 offset:672\n\t"
        "ds_read_b32 v9, v30 offset:704\n\t"
        "v_lshlrev_b32 v31, 4, v1\n\t"
        "v_and_b32 v31, 0x70, v31\n\t"
        "v_add_u32 v31, 3072, v31\n\t"
        "s_waitcnt lgkmcnt(0)\n\t"
        "v_add_u32 v4, v4, v31\n\t"
        "v_add_u32 v5, v5, v31\n\t"
        "v_add_u32 v6, v6, v31\n\t"
        "v_add_u32 v7, v7, v31\n\t"
        "v_add_u32 v8, v8, v31\n\t"
        "v_add_u32 v9, v9, v31\n\t"
        "s_cmp_le_u32 s37, 32\n\t"
        "s_cbranch_scc1 .Ldin_xd0\n\t"
        "s_add_u32 m0, s31, 4096\n\t"
        "s_nop 0\n\t"
        "buffer_load_dwordx4 v4, s[40:43], s6 offen lds\n\t"
        "s_cmp_le_u32 s37, 40\n\t"
        "s_cbranch_scc1 .Ldin_xd0\n\t"
        "s_add_u32 m0, s31, 5120\n\t"
        "s_nop 0\n\t"
        "buffer_load_dwordx4 v5, s[40:43], s6 offen lds\n\t"
        "s_cmp_le_u32 s37, 48\n\t"
        "s_cbranch_scc1 .Ldin_xd0\n\t"
        "s_add_u32 m0, s31, 6144\n\t"
        "s_mov_b64 exec, 0xffff\n\t"
        "buffer_load_dwordx4 v6, s[40:43], s6 offen lds\n\t"
        "s_mov_b64 exec, -1\n\t"
        ".Ldin_xd0:\n\t"
        "s_cmp_le_u32 s38, 32\n\t"
        "s_cbranch_scc1 .Ldin_xd1\n\t"
        "s_add_u32 m0, s31, 10496\n\t"
        "s_nop 0\n\t"
        "buffer_load_dwordx4 v7, s[40:43], s7 offen lds\n\t"
        "s_cmp_le_u32 s38, 40\n\t"
        "s_cbranch_scc1 .Ldin_xd1\n\t"
        "s_add_u32 m0, s31, 11520\n\t"
        "s_nop 0\n\t"
        "buffer_load_dwordx4 v8, s[40:43], s7 offen lds\n\t"
        "s_cmp_le_u32 s38, 48\n\t"
        "s_cbranch_scc1 .Ldin_xd1\n\t"
        "s_add_u32 m0, s31, 12544\n\t"
        "s_mov_b64 exec, 0xffff\n\t"
        "buffer_load_dwordx4 v9, s[40:43], s7 offen lds\n\t"
        "s_mov_b64 exec, -1\n\t"
        ".Ldin_xd1:\n\t"
        "s_branch .Ldin_xdma_ret\n\t"
        ".Ldin_big:\n\t"
        "s_mov_b32 s16, 32\n\t"
        "s_mov_b32 s20, 32\n\t"
        "s_mov_b32 s21, 32\n\t"
        "s_cmp_le_u32 s37, 32\n\t"
        "s_cbranch_scc1 .Ldin_big1\n\t"
        ".Ldin_big0:\n\t"
        "s_mov_b32 s20, 40\n\t"
        "s_lshl_b32 s17, s16, 7\n\t"
        "v_mov_b32 v9, 44032\n\t"
        "s_add_u32 s17, s17, s31\n\t"
        "v_and_b32 v8, 7, v1\n\t"
        "v_lshrrev_b32 v140, 1, v1\n\t"
        "v_and_b32 v140, 16, v140\n\t"
        "v_lshl_or_b32 v137, v8, 7, v140\n\t"
        "v_add_u32 v137, s17, v137\n\t"
        "v_and_b32 v139, 0x60, v29\n\t"
        "v_or_b32 v140, v139, v140\n\t"
        "v_bfe_u32 v139, v1, 2, 3\n\t"
        "v_lshlrev_b32 v136, 3, v1\n\t"
        "v_and_b32 v138, 24, v136\n\t"
        "v_lshl_or_b32 v138, v139, 7, v138\n\t"
        "v_add_u32 v138, s17, v138\n\t"
        "v_and_b32 v139, 0x80, v136\n\t"
        "v_add_u32 v139, v139, v11\n\t"
        "v_cndmask_b32 v136, v137, v9, s[46:47]\n\t"
        "v_cndmask_b32 v137, v9, v137, s[46:47]\n\t"
        "s_lshl2_add_u32 s18, s16, s30\n\t"
        "s_add_u32 s18, s18, 256\n\t"
        "v_and_b32 v8, 16, v1\n\t"
        "v_add_u32 v8, s18, v8\n\t"
        "s_mov_b32 s76, 0x00ff00ff\n\t"
        "s_mov_b32 s77, 0xff00ff00\n\t"
        "v_cndmask_b32 v8, v9, v8, s[76:77]\n\t"
        "s_mov_b32 s76, 0xff00ff00\n\t"
        "ds_read_b128 v[224:227], v139 offset:44288\n\t"
        "ds_read_b128 v[228:231], v139 offset:44304\n\t"
        "ds_read_b128 v[232:235], v139 offset:44352\n\t"
        "ds_read_b128 v[236:239], v139 offset:44368\n\t"
        "ds_read_b128 v[144:147], v136\n\t"
        "ds_read_b128 v[148:151], v137\n\t"
        "ds_read_b128 v[152:155], v136 offset:32\n\t"
        "ds_read_b128 v[156:159], v137 offset:32\n\t"
        "ds_read_b128 v[160:163], v136 offset:64\n\t"
        "ds_read_b128 v[164:167], v137 offset:64\n\t"
        "ds_read_b128 v[168:171], v136 offset:96\n\t"
        "ds_read_b128 v[172:175], v137 offset:96\n\t"
        "v_exp_f32 v211, v211\n\t"
        "v_exp_f32 v212, v212\n\t"
        "v_exp_f32 v213, v213\n\t"
        "v_exp_f32 v214, v214\n\t"
        "ds_read_b128 v[184:187], v140 offset:43264\n\t"
        "ds_read_b128 v[240:243], v8\n\t"
        "ds_read_b64_tr_b16 v[32:33], v138\n\t"
        "ds_read_b64_tr_b16 v[34:35], v138\n\t"
        "ds_read_b64_tr_b16 v[36:37], v138 offset:32\n\t"
        "ds_read_b64_tr_b16 v[38:39], v138 offset:32\n\t"
        "ds_read_b64_tr_b16 v[40:41], v138 offset:64\n\t"
        "ds_read_b64_tr_b16 v[42:43], v138 offset:64\n\t"
        "ds_read_b64_tr_b16 v[44:45], v138 offset:96\n\t"
        "ds_read_b64_tr_b16 v[46:47], v138 offset:96\n\t"
        "v_exp_f32 v215, v215\n\t"
        "v_exp_f32 v216, v216\n\t"
        "v_exp_f32 v217, v217\n\t"
        "v_pk_add_f32 v[208:209], v[208:209], 1.0 op_sel_hi:[1,0]\n\t"
        "v_pk_add_f32 v[210:211], v[210:211], 1.0 op_sel_hi:[1,0]\n\t"
        "v_pk_add_f32 v[212:213], v[212:213], 1.0 op_sel_hi:[1,0]\n\t"
        "v_pk_add_f32 v[214:215], v[214:215], 1.0 op_sel_hi:[1,0]\n\t"
        "v_exp_f32 v218, v218\n\t"
        "s_waitcnt lgkmcnt(15)\n\t"
        "v_mfma_f32_32x32x16_bf16 v[224:239], v[64:67], v[144:147], v[224:239]\n\t"
        "v_exp_f32 v219, v219\n\t"
        "v_exp_f32 v220, v220\n\t"
        "v_exp_f32 v221, v221\n\t"
        "v_mfma_f32_32x32x16_bf16 v[224:239], v[80:83], v[148:151], v[224:239]\n\t"
        "v_exp_f32 v222, v222\n\t"
        "v_exp_f32 v223, v223\n\t"
        "v_rcp_f32 v208, v208\n\t"
        "v_rcp_f32 v209, v209\n\t"
        "v_pk_add_f32 v[216:217], v[216:217], 1.0 op_sel_hi:[1,0]\n\t"
        "v_pk_add_f32 v[218:219], v[218:219], 1.0 op_sel_hi:[1,0]\n\t"
        "v_pk_add_f32 v[220:221], v[220:221], 1.0 op_sel_hi:[1,0]\n\t"
        "v_pk_add_f32 v[222:223], v[222:223], 1.0 op_sel_hi:[1,0]\n\t"
        "v_mfma_f32_32x32x16_bf16 v[224:239], v[68:71], v[152:155], v[224:239]\n\t"
        "v_rcp_f32 v210, v210\n\t"
        "v_rcp_f32 v211, v211\n\t"
        "v_rcp_f32 v212, v212\n\t"
        "s_waitcnt lgkmcnt(14)\n\t"
        "v_mfma_f32_32x32x16_bf16 v[224:239], v[84:87], v[156:159], v[224:239]\n\t"
        "v_rcp_f32 v213, v213\n\t"
        "v_rcp_f32 v214, v214\n\t"
        "v_rcp_f32 v215, v215\n\t"
        "s_waitcnt lgkmcnt(13)\n\t"
        "v_mfma_f32_32x32x16_bf16 v[224:239], v[72:75], v[160:163], v[224:239]\n\t"
        "v_rcp_f32 v216, v216\n\t"
        "v_rcp_f32 v217, v217\n\t"
        "v_cvt_pk_bf16_f32 v208, v208, v209\n\t"
        "s_waitcnt lgkmcnt(12)\n\t"
        "v_mfma_f32_32x32x16_bf16 v[224:239], v[88:91], v[164:167], v[224:239]\n\t"
        "v_cvt_pk_bf16_f32 v209, v210, v211\n\t"
        "v_cvt_pk_bf16_f32 v210, v212, v213\n\t"
        "v_cvt_pk_bf16_f32 v211, v214, v215\n\t"
        "v_rcp_f32 v218, v218\n\t"
        "v_rcp_f32 v219, v219\n\t"
        "v_mfma_f32_16x16x32_bf16 v[128:131], v[208:211], v[248:251], v[128:131]\n\t"
        "v_rcp_f32 v220, v220\n\t"
        "s_waitcnt lgkmcnt(11)\n\t"
        "v_mfma_f32_32x32x16_bf16 v[224:239], v[76:79], v[168:171], v[224:239]\n\t"
        "v_rcp_f32 v221, v221\n\t"
        "v_rcp_f32 v222, v222\n\t"
        "v_rcp_f32 v223, v223\n\t"
        "s_waitcnt lgkmcnt(10)\n\t"
        "v_mfma_f32_32x32x16_bf16 v[224:239], v[92:95], v[172:175], v[224:239]\n\t"
        "v_cvt_pk_bf16_f32 v216, v216, v217\n\t"
        "v_cvt_pk_bf16_f32 v217, v218, v219\n\t"
        "v_cvt_pk_bf16_f32 v218, v220, v221\n\t"
        "v_cvt_pk_bf16_f32 v219, v222, v223\n\t"
        "s_nop 1\n\t"
        "v_mfma_f32_16x16x32_bf16 v[128:131], v[216:219], v[252:255], v[128:131]\n\t"
        "s_setprio 0\n\t"
        "s_cmp_gt_u32 s38, 32\n\t"
        "s_cbranch_scc1 .Ldin_both\n\t"
        "s_nop 1\n\t"
        "v_cndmask_b32 v224, v224, v232, s[76:77]\n\t"
        "v_cndmask_b32 v225, v225, v233, s[76:77]\n\t"
        "v_cndmask_b32 v226, v226, v234, s[76:77]\n\t"
        "v_pk_mul_f32 v[128:129], v[128:129], v[180:181]\n\t"
        "v_pk_mul_f32 v[130:131], v[130:131], v[182:183]\n\t"
        "v_cndmask_b32 v227, v227, v235, s[76:77]\n\t"
        "v_cndmask_b32 v228, v228, v236, s[76:77]\n\t"
        "v_cvt_pk_bf16_f32 v132, v128, v129\n\t"
        "v_cvt_pk_bf16_f32 v133, v130, v131\n\t"
        "v_cndmask_b32 v229, v229, v237, s[76:77]\n\t"
        "v_cndmask_b32 v230, v230, v238, s[76:77]\n\t"
        "v_cndmask_b32 v231, v231, v239, s[76:77]\n\t"
        "v_mov_b32_dpp v134, v132 row_ror:8 row_mask:0xf bank_mask:0xf\n\t"
        "v_mov_b32_dpp v135, v133 row_ror:8 row_mask:0xf bank_mask:0xf\n\t"
        "v_exp_f32 v224, v224\n\t"
        "v_exp_f32 v225, v225\n\t"
        "v_mfma_f32_16x16x32_bf16 v[192:195], v[132:135], v[48:51], 0\n\t"
        "v_exp_f32 v226, v226\n\t"
        "v_mfma_f32_16x16x32_bf16 v[196:199], v[132:135], v[52:55], 0\n\t"
        "v_exp_f32 v227, v227\n\t"
        "v_mfma_f32_16x16x32_bf16 v[200:203], v[132:135], v[56:59], 0\n\t"
        "v_exp_f32 v228, v228\n\t"
        "v_mfma_f32_16x16x32_bf16 v[204:207], v[132:135], v[60:63], 0\n\t"
        "v_exp_f32 v229, v229\n\t"
        "v_exp_f32 v230, v230\n\t"
        "v_exp_f32 v231, v231\n\t"
        "v_pk_mul_f32 v[244:245], v[24:25], 0.5 op_sel_hi:[1,0]\n\t"
        "v_pk_mul_f32 v[246:247], v[26:27], 0.5 op_sel_hi:[1,0]\n\t"
        "v_pk_add_f32 v[224:225], v[224:225], 1.0 op_sel_hi:[1,0]\n\t"
        "v_pk_add_f32 v[226:227], v[226:227], 1.0 op_sel_hi:[1,0]\n\t"
        "v_pk_add_f32 v[228:229], v[228:229], 1.0 op_sel_hi:[1,0]\n\t"
        "v_pk_add_f32 v[230:231], v[230:231], 1.0 op_sel_hi:[1,0]\n\t"
        "v_cndmask_b32 v23, v192, v196, s[46:47]\n\t"
        "v_rcp_f32 v224, v224\n\t"
        "v_rcp_f32 v225, v225\n\t"
        "v_cndmask_b32 v30, v200, v204, s[46:47]\n\t"
        "v_rcp_f32 v226, v226\n\t"
        "v_mov_b32 v134, 0\n\t"
        "v_mov_b32 v135, 0\n\t"
        "v_rcp_f32 v227, v227\n\t"
        "v_rcp_f32 v228, v228\n\t"
        "v_permlane32_swap_b32 v23, v30\n\t"
        "v_rcp_f32 v229, v229\n\t"
        "v_rcp_f32 v230, v230\n\t"
        "v_cvt_pk_bf16_f32 v31, v23, v23\n\t"
        "global_store_short v2, v31, s[14:15] offset:128\n\t"
        "v_rcp_f32 v231, v231\n\t"
        "v_cvt_pk_bf16_f32 v224, v224, v225\n\t"
        "v_cvt_pk_bf16_f32 v225, v226, v227\n\t"
        "v_cvt_pk_bf16_f32 v226, v228, v229\n\t"
        "v_cvt_pk_bf16_f32 v227, v230, v231\n\t"
        "s_waitcnt lgkmcnt(9)\n\t"
        "s_nop 0\n\t"
        "v_mfma_f32_16x16x32_bf16 v[244:247], v[224:227], v[184:187], v[244:247]\n\t"
        "s_waitcnt lgkmcnt(8)\n\t"
        "s_nop 6\n\t"
        "v_pk_mul_f32 v[244:245], v[244:245], v[240:241]\n\t"
        "v_pk_mul_f32 v[246:247], v[246:247], v[242:243]\n\t"
        "v_cvt_pk_bf16_f32 v132, v244, v245\n\t"
        "v_cvt_pk_bf16_f32 v133, v246, v247\n\t"
        "s_waitcnt lgkmcnt(6)\n\t"
        "s_nop 0\n\t"
        "v_mfma_f32_16x16x32_bf16 v[224:227], v[132:135], v[32:35], 0\n\t"
        "s_waitcnt lgkmcnt(4)\n\t"
        "v_mfma_f32_16x16x32_bf16 v[228:231], v[132:135], v[36:39], 0\n\t"
        "s_waitcnt lgkmcnt(2)\n\t"
        "v_mfma_f32_16x16x32_bf16 v[232:235], v[132:135], v[40:43], 0\n\t"
        "s_waitcnt lgkmcnt(0)\n\t"
        "v_mfma_f32_16x16x32_bf16 v[236:239], v[132:135], v[44:47], 0\n\t"
        "s_nop 3\n\t"
        "v_cndmask_b32 v8, v224, v228, s[46:47]\n\t"
        "s_nop 2\n\t"
        "v_cndmask_b32 v140, v232, v236, s[46:47]\n\t"
        "s_nop 1\n\t"
        "v_permlane32_swap_b32 v8, v140\n\t"
        "v_add_f32 v22, v22, v8\n\t"
        "v_add_f32 v22, v22, v140\n\t"
        "v_cvt_pk_bf16_f32 v8, v22, v22\n\t"
        "global_store_short v2, v8, s[14:15]\n\t"
        "s_branch .Ldin_extra\n\t"
        ".Ldin_big1:\n\t"
        "s_mov_b32 s21, 40\n\t"
        "s_lshl_b32 s17, s16, 7\n\t"
        "v_mov_b32 v9, 44032\n\t"
        "s_add_u32 s17, s17, s3\n\t"
        "v_and_b32 v8, 7, v1\n\t"
        "v_lshrrev_b32 v140, 1, v1\n\t"
        "v_and_b32 v140, 16, v140\n\t"
        "v_lshl_or_b32 v137, v8, 7, v140\n\t"
        "v_add_u32 v137, s17, v137\n\t"
        "v_and_b32 v139, 0x60, v29\n\t"
        "v_or_b32 v140, v139, v140\n\t"
        "v_bfe_u32 v139, v1, 2, 3\n\t"
        "v_lshlrev_b32 v136, 3, v1\n\t"
        "v_and_b32 v138, 24, v136\n\t"
        "v_lshl_or_b32 v138, v139, 7, v138\n\t"
        "v_add_u32 v138, s17, v138\n\t"
        "v_and_b32 v139, 0x80, v136\n\t"
        "v_add_u32 v139, v139, v11\n\t"
        "v_cndmask_b32 v136, v137, v9, s[46:47]\n\t"
        "v_cndmask_b32 v137, v9, v137, s[46:47]\n\t"
        "s_lshl2_add_u32 s18, s16, s30\n\t"
        "s_add_u32 s18, s18, 768\n\t"
        "v_and_b32 v8, 16, v1\n\t"
        "v_add_u32 v8, s18, v8\n\t"
        "s_mov_b32 s76, 0x00ff00ff\n\t"
        "s_mov_b32 s77, 0xff00ff00\n\t"
        "v_cndmask_b32 v8, v9, v8, s[76:77]\n\t"
        "s_mov_b32 s76, 0xff00ff00\n\t"
        "ds_read_b128 v[224:227], v139 offset:44560\n\t"
        "ds_read_b128 v[228:231], v139 offset:44576\n\t"
        "ds_read_b128 v[232:235], v139 offset:44624\n\t"
        "ds_read_b128 v[236:239], v139 offset:44640\n\t"
        "ds_read_b128 v[144:147], v136\n\t"
        "ds_read_b128 v[148:151], v137\n\t"
        "ds_read_b128 v[152:155], v136 offset:32\n\t"
        "ds_read_b128 v[156:159], v137 offset:32\n\t"
        "ds_read_b128 v[160:163], v136 offset:64\n\t"
        "ds_read_b128 v[164:167], v137 offset:64\n\t"
        "ds_read_b128 v[168:171], v136 offset:96\n\t"
        "ds_read_b128 v[172:175], v137 offset:96\n\t"
        "v_exp_f32 v211, v211\n\t"
        "v_exp_f32 v212, v212\n\t"
        "v_exp_f32 v213, v213\n\t"
        "v_exp_f32 v214, v214\n\t"
        "ds_read_b128 v[184:187], v140 offset:43264\n\t"
        "ds_read_b128 v[240:243], v8\n\t"
        "ds_read_b64_tr_b16 v[32:33], v138\n\t"
        "ds_read_b64_tr_b16 v[34:35], v138\n\t"
        "ds_read_b64_tr_b16 v[36:37], v138 offset:32\n\t"
        "ds_read_b64_tr_b16 v[38:39], v138 offset:32\n\t"
        "ds_read_b64_tr_b16 v[40:41], v138 offset:64\n\t"
        "ds_read_b64_tr_b16 v[42:43], v138 offset:64\n\t"
        "ds_read_b64_tr_b16 v[44:45], v138 offset:96\n\t"
        "ds_read_b64_tr_b16 v[46:47], v138 offset:96\n\t"
        "v_exp_f32 v215, v215\n\t"
        "v_exp_f32 v216, v216\n\t"
        "v_exp_f32 v217, v217\n\t"
        "v_pk_add_f32 v[208:209], v[208:209], 1.0 op_sel_hi:[1,0]\n\t"
        "v_pk_add_f32 v[210:211], v[210:211], 1.0 op_sel_hi:[1,0]\n\t"
        "v_pk_add_f32 v[212:213], v[212:213], 1.0 op_sel_hi:[1,0]\n\t"
        "v_pk_add_f32 v[214:215], v[214:215], 1.0 op_sel_hi:[1,0]\n\t"
        "v_exp_f32 v218, v218\n\t"
        "s_waitcnt lgkmcnt(15)\n\t"
        "v_mfma_f32_32x32x16_bf16 v[224:239], v[96:99], v[144:147], v[224:239]\n\t"
        "v_exp_f32 v219, v219\n\t"
        "v_exp_f32 v220, v220\n\t"
        "v_exp_f32 v221, v221\n\t"
        "v_mfma_f32_32x32x16_bf16 v[224:239], v[112:115], v[148:151], v[224:239]\n\t"
        "v_exp_f32 v222, v222\n\t"
        "v_exp_f32 v223, v223\n\t"
        "v_rcp_f32 v208, v208\n\t"
        "v_rcp_f32 v209, v209\n\t"
        "v_pk_add_f32 v[216:217], v[216:217], 1.0 op_sel_hi:[1,0]\n\t"
        "v_pk_add_f32 v[218:219], v[218:219], 1.0 op_sel_hi:[1,0]\n\t"
        "v_pk_add_f32 v[220:221], v[220:221], 1.0 op_sel_hi:[1,0]\n\t"
        "v_pk_add_f32 v[222:223], v[222:223], 1.0 op_sel_hi:[1,0]\n\t"
        "v_mfma_f32_32x32x16_bf16 v[224:239], v[100:103], v[152:155], v[224:239]\n\t"
        "v_rcp_f32 v210, v210\n\t"
        "v_rcp_f32 v211, v211\n\t"
        "v_rcp_f32 v212, v212\n\t"
        "s_waitcnt lgkmcnt(14)\n\t"
        "v_mfma_f32_32x32x16_bf16 v[224:239], v[116:119], v[156:159], v[224:239]\n\t"
        "v_rcp_f32 v213, v213\n\t"
        "v_rcp_f32 v214, v214\n\t"
        "v_rcp_f32 v215, v215\n\t"
        "s_waitcnt lgkmcnt(13)\n\t"
        "v_mfma_f32_32x32x16_bf16 v[224:239], v[104:107], v[160:163], v[224:239]\n\t"
        "v_rcp_f32 v216, v216\n\t"
        "v_rcp_f32 v217, v217\n\t"
        "v_cvt_pk_bf16_f32 v208, v208, v209\n\t"
        "s_waitcnt lgkmcnt(12)\n\t"
        "v_mfma_f32_32x32x16_bf16 v[224:239], v[120:123], v[164:167], v[224:239]\n\t"
        "v_cvt_pk_bf16_f32 v209, v210, v211\n\t"
        "v_cvt_pk_bf16_f32 v210, v212, v213\n\t"
        "v_cvt_pk_bf16_f32 v211, v214, v215\n\t"
        "v_rcp_f32 v218, v218\n\t"
        "v_rcp_f32 v219, v219\n\t"
        "v_mfma_f32_16x16x32_bf16 v[128:131], v[208:211], v[248:251], v[128:131]\n\t"
        "v_rcp_f32 v220, v220\n\t"
        "s_waitcnt lgkmcnt(11)\n\t"
        "v_mfma_f32_32x32x16_bf16 v[224:239], v[108:111], v[168:171], v[224:239]\n\t"
        "v_rcp_f32 v221, v221\n\t"
        "v_rcp_f32 v222, v222\n\t"
        "v_rcp_f32 v223, v223\n\t"
        "s_waitcnt lgkmcnt(10)\n\t"
        "v_mfma_f32_32x32x16_bf16 v[224:239], v[124:127], v[172:175], v[224:239]\n\t"
        "v_cvt_pk_bf16_f32 v216, v216, v217\n\t"
        "v_cvt_pk_bf16_f32 v217, v218, v219\n\t"
        "v_cvt_pk_bf16_f32 v218, v220, v221\n\t"
        "v_cvt_pk_bf16_f32 v219, v222, v223\n\t"
        "s_nop 1\n\t"
        "v_mfma_f32_16x16x32_bf16 v[128:131], v[216:219], v[252:255], v[128:131]\n\t"
        "s_setprio 0\n\t"
        "s_nop 3\n\t"
        "v_cndmask_b32 v224, v224, v232, s[76:77]\n\t"
        "v_cndmask_b32 v225, v225, v233, s[76:77]\n\t"
        "v_cndmask_b32 v226, v226, v234, s[76:77]\n\t"
        "v_pk_mul_f32 v[128:129], v[128:129], v[180:181]\n\t"
        "v_pk_mul_f32 v[130:131], v[130:131], v[182:183]\n\t"
        "v_cndmask_b32 v227, v227, v235, s[76:77]\n\t"
        "v_cndmask_b32 v228, v228, v236, s[76:77]\n\t"
        "v_cvt_pk_bf16_f32 v132, v128, v129\n\t"
        "v_cvt_pk_bf16_f32 v133, v130, v131\n\t"
        "v_cndmask_b32 v229, v229, v237, s[76:77]\n\t"
        "v_cndmask_b32 v230, v230, v238, s[76:77]\n\t"
        "v_cndmask_b32 v231, v231, v239, s[76:77]\n\t"
        "v_mov_b32_dpp v134, v132 row_ror:8 row_mask:0xf bank_mask:0xf\n\t"
        "v_mov_b32_dpp v135, v133 row_ror:8 row_mask:0xf bank_mask:0xf\n\t"
        "v_exp_f32 v224, v224\n\t"
        "v_exp_f32 v225, v225\n\t"
        "v_mfma_f32_16x16x32_bf16 v[192:195], v[132:135], v[48:51], 0\n\t"
        "v_exp_f32 v226, v226\n\t"
        "v_mfma_f32_16x16x32_bf16 v[196:199], v[132:135], v[52:55], 0\n\t"
        "v_exp_f32 v227, v227\n\t"
        "v_mfma_f32_16x16x32_bf16 v[200:203], v[132:135], v[56:59], 0\n\t"
        "v_exp_f32 v228, v228\n\t"
        "v_mfma_f32_16x16x32_bf16 v[204:207], v[132:135], v[60:63], 0\n\t"
        "v_exp_f32 v229, v229\n\t"
        "v_exp_f32 v230, v230\n\t"
        "v_exp_f32 v231, v231\n\t"
        "v_pk_mul_f32 v[244:245], v[24:25], 0.5 op_sel_hi:[1,0]\n\t"
        "v_pk_mul_f32 v[246:247], v[26:27], 0.5 op_sel_hi:[1,0]\n\t"
        "v_pk_add_f32 v[224:225], v[224:225], 1.0 op_sel_hi:[1,0]\n\t"
        "v_pk_add_f32 v[226:227], v[226:227], 1.0 op_sel_hi:[1,0]\n\t"
        "v_pk_add_f32 v[228:229], v[228:229], 1.0 op_sel_hi:[1,0]\n\t"
        "v_pk_add_f32 v[230:231], v[230:231], 1.0 op_sel_hi:[1,0]\n\t"
        "v_cndmask_b32 v23, v192, v196, s[46:47]\n\t"
        "v_rcp_f32 v224, v224\n\t"
        "v_rcp_f32 v225, v225\n\t"
        "v_cndmask_b32 v30, v200, v204, s[46:47]\n\t"
        "v_rcp_f32 v226, v226\n\t"
        "v_mov_b32 v134, 0\n\t"
        "v_mov_b32 v135, 0\n\t"
        "v_rcp_f32 v227, v227\n\t"
        "v_rcp_f32 v228, v228\n\t"
        "v_permlane32_swap_b32 v23, v30\n\t"
        "v_rcp_f32 v229, v229\n\t"
        "v_rcp_f32 v230, v230\n\t"
        "v_cvt_pk_bf16_f32 v31, v23, v23\n\t"
        "global_store_short v2, v31, s[14:15] offset:128\n\t"
        "v_rcp_f32 v231, v231\n\t"
        "v_cvt_pk_bf16_f32 v224, v224, v225\n\t"
        "v_cvt_pk_bf16_f32 v225, v226, v227\n\t"
        "v_cvt_pk_bf16_f32 v226, v228, v229\n\t"
        "v_cvt_pk_bf16_f32 v227, v230, v231\n\t"
        "s_waitcnt lgkmcnt(9)\n\t"
        "s_nop 0\n\t"
        "v_mfma_f32_16x16x32_bf16 v[244:247], v[224:227], v[184:187], v[244:247]\n\t"
        "s_waitcnt lgkmcnt(8)\n\t"
        "s_nop 6\n\t"
        "v_pk_mul_f32 v[244:245], v[244:245], v[240:241]\n\t"
        "v_pk_mul_f32 v[246:247], v[246:247], v[242:243]\n\t"
        "v_cvt_pk_bf16_f32 v132, v244, v245\n\t"
        "v_cvt_pk_bf16_f32 v133, v246, v247\n\t"
        "s_waitcnt lgkmcnt(6)\n\t"
        "s_nop 0\n\t"
        "v_mfma_f32_16x16x32_bf16 v[224:227], v[132:135], v[32:35], 0\n\t"
        "s_waitcnt lgkmcnt(4)\n\t"
        "v_mfma_f32_16x16x32_bf16 v[228:231], v[132:135], v[36:39], 0\n\t"
        "s_waitcnt lgkmcnt(2)\n\t"
        "v_mfma_f32_16x16x32_bf16 v[232:235], v[132:135], v[40:43], 0\n\t"
        "s_waitcnt lgkmcnt(0)\n\t"
        "v_mfma_f32_16x16x32_bf16 v[236:239], v[132:135], v[44:47], 0\n\t"
        "s_nop 3\n\t"
        "v_cndmask_b32 v8, v224, v228, s[46:47]\n\t"
        "s_nop 2\n\t"
        "v_cndmask_b32 v140, v232, v236, s[46:47]\n\t"
        "s_nop 1\n\t"
        "v_permlane32_swap_b32 v8, v140\n\t"
        "v_add_f32 v23, v23, v8\n\t"
        "v_add_f32 v23, v23, v140\n\t"
        "v_cvt_pk_bf16_f32 v8, v23, v23\n\t"
        "global_store_short v2, v8, s[14:15] offset:128\n\t"
        "s_branch .Ldin_extra\n\t"
        ".Ldin_both:\n\t"
        "s_mov_b32 s21, 40\n\t"
        "s_lshl_b32 s17, s16, 7\n\t"
        "v_mov_b32 v9, 44032\n\t"
        "s_add_u32 s17, s17, s3\n\t"
        "v_and_b32 v8, 7, v1\n\t"
        "v_lshrrev_b32 v140, 1, v1\n\t"
        "v_and_b32 v140, 16, v140\n\t"
        "v_lshl_or_b32 v137, v8, 7, v140\n\t"
        "v_add_u32 v137, s17, v137\n\t"
        "v_and_b32 v139, 0x60, v29\n\t"
        "v_or_b32 v140, v139, v140\n\t"
        "v_bfe_u32 v139, v1, 2, 3\n\t"
        "v_lshlrev_b32 v136, 3, v1\n\t"
        "v_and_b32 v138, 24, v136\n\t"
        "v_lshl_or_b32 v138, v139, 7, v138\n\t"
        "v_add_u32 v138, s17, v138\n\t"
        "v_and_b32 v139, 0x80, v136\n\t"
        "v_add_u32 v139, v139, v11\n\t"
        "v_cndmask_b32 v136, v137, v9, s[46:47]\n\t"
        "v_cndmask_b32 v137, v9, v137, s[46:47]\n\t"
        "s_lshl2_add_u32 s18, s16, s30\n\t"
        "s_add_u32 s18, s18, 768\n\t"
        "v_and_b32 v8, 16, v1\n\t"
        "v_add_u32 v8, s18, v8\n\t"
        "s_mov_b32 s76, 0x00ff00ff\n\t"
        "s_mov_b32 s77, 0xff00ff00\n\t"
        "v_cndmask_b32 v8, v9, v8, s[76:77]\n\t"
        "s_mov_b32 s76, 0xff00ff00\n\t"
        "ds_read_b128 v[208:211], v139 offset:44560\n\t"
        "ds_read_b128 v[212:215], v139 offset:44576\n\t"
        "ds_read_b128 v[216:219], v139 offset:44624\n\t"
        "ds_read_b128 v[220:223], v139 offset:44640\n\t"
        "ds_read_b128 v[144:147], v136\n\t"
        "ds_read_b128 v[148:151], v137\n\t"
        "ds_read_b128 v[152:155], v136 offset:32\n\t"
        "ds_read_b128 v[156:159], v137 offset:32\n\t"
        "ds_read_b128 v[160:163], v136 offset:64\n\t"
        "ds_read_b128 v[164:167], v137 offset:64\n\t"
        "ds_read_b128 v[168:171], v136 offset:96\n\t"
        "ds_read_b128 v[172:175], v137 offset:96\n\t"
        "ds_read_b128 v[248:251], v8\n\t"
        "v_cndmask_b32 v224, v224, v232, s[76:77]\n\t"
        "v_cndmask_b32 v225, v225, v233, s[76:77]\n\t"
        "v_cndmask_b32 v226, v226, v234, s[76:77]\n\t"
        "v_pk_mul_f32 v[128:129], v[128:129], v[180:181]\n\t"
        "v_pk_mul_f32 v[130:131], v[130:131], v[182:183]\n\t"
        "v_cndmask_b32 v227, v227, v235, s[76:77]\n\t"
        "v_cndmask_b32 v228, v228, v236, s[76:77]\n\t"
        "v_cvt_pk_bf16_f32 v132, v128, v129\n\t"
        "v_cvt_pk_bf16_f32 v133, v130, v131\n\t"
        "v_cndmask_b32 v229, v229, v237, s[76:77]\n\t"
        "v_cndmask_b32 v230, v230, v238, s[76:77]\n\t"
        "v_cndmask_b32 v231, v231, v239, s[76:77]\n\t"
        "v_mov_b32_dpp v134, v132 row_ror:8 row_mask:0xf bank_mask:0xf\n\t"
        "v_mov_b32_dpp v135, v133 row_ror:8 row_mask:0xf bank_mask:0xf\n\t"
        "v_exp_f32 v224, v224\n\t"
        "v_exp_f32 v225, v225\n\t"
        "v_mfma_f32_16x16x32_bf16 v[192:195], v[132:135], v[48:51], 0\n\t"
        "v_exp_f32 v226, v226\n\t"
        "v_mfma_f32_16x16x32_bf16 v[196:199], v[132:135], v[52:55], 0\n\t"
        "v_exp_f32 v227, v227\n\t"
        "v_mfma_f32_16x16x32_bf16 v[200:203], v[132:135], v[56:59], 0\n\t"
        "v_exp_f32 v228, v228\n\t"
        "v_mfma_f32_16x16x32_bf16 v[204:207], v[132:135], v[60:63], 0\n\t"
        "v_exp_f32 v229, v229\n\t"
        "v_exp_f32 v230, v230\n\t"
        "v_exp_f32 v231, v231\n\t"
        "ds_read_b64_tr_b16 v[48:49], v138\n\t"
        "ds_read_b64_tr_b16 v[50:51], v138\n\t"
        "ds_read_b64_tr_b16 v[52:53], v138 offset:32\n\t"
        "ds_read_b64_tr_b16 v[54:55], v138 offset:32\n\t"
        "ds_read_b64_tr_b16 v[56:57], v138 offset:64\n\t"
        "ds_read_b64_tr_b16 v[58:59], v138 offset:64\n\t"
        "ds_read_b64_tr_b16 v[60:61], v138 offset:96\n\t"
        "ds_read_b64_tr_b16 v[62:63], v138 offset:96\n\t"
        "v_pk_mul_f32 v[244:245], v[24:25], 0.5 op_sel_hi:[1,0]\n\t"
        "v_pk_mul_f32 v[246:247], v[26:27], 0.5 op_sel_hi:[1,0]\n\t"
        "v_pk_add_f32 v[224:225], v[224:225], 1.0 op_sel_hi:[1,0]\n\t"
        "v_pk_add_f32 v[226:227], v[226:227], 1.0 op_sel_hi:[1,0]\n\t"
        "v_pk_add_f32 v[228:229], v[228:229], 1.0 op_sel_hi:[1,0]\n\t"
        "v_pk_add_f32 v[230:231], v[230:231], 1.0 op_sel_hi:[1,0]\n\t"
        "v_cndmask_b32 v23, v192, v196, s[46:47]\n\t"
        "v_rcp_f32 v224, v224\n\t"
        "s_waitcnt lgkmcnt(15)\n\t"
        "v_mfma_f32_32x32x16_bf16 v[208:223], v[96:99], v[144:147], v[208:223]\n\t"
        "v_rcp_f32 v225, v225\n\t"
        "v_cndmask_b32 v30, v200, v204, s[46:47]\n\t"
        "v_rcp_f32 v226, v226\n\t"
        "v_mov_b32 v134, 0\n\t"
        "v_mov_b32 v135, 0\n\t"
        "v_rcp_f32 v227, v227\n\t"
        "v_mfma_f32_32x32x16_bf16 v[208:223], v[112:115], v[148:151], v[208:223]\n\t"
        "v_rcp_f32 v228, v228\n\t"
        "v_permlane32_swap_b32 v23, v30\n\t"
        "v_rcp_f32 v229, v229\n\t"
        "v_rcp_f32 v230, v230\n\t"
        "v_cvt_pk_bf16_f32 v31, v23, v23\n\t"
        "global_store_short v2, v31, s[14:15] offset:128\n\t"
        "v_rcp_f32 v231, v231\n\t"
        "s_waitcnt lgkmcnt(14)\n\t"
        "v_mfma_f32_32x32x16_bf16 v[208:223], v[100:103], v[152:155], v[208:223]\n\t"
        "v_cvt_pk_bf16_f32 v224, v224, v225\n\t"
        "v_cvt_pk_bf16_f32 v225, v226, v227\n\t"
        "v_cvt_pk_bf16_f32 v226, v228, v229\n\t"
        "v_cvt_pk_bf16_f32 v227, v230, v231\n\t"
        "s_nop 1\n\t"
        "v_mfma_f32_16x16x32_bf16 v[244:247], v[224:227], v[184:187], v[244:247]\n\t"
        "s_waitcnt lgkmcnt(13)\n\t"
        "v_mfma_f32_32x32x16_bf16 v[208:223], v[116:119], v[156:159], v[208:223]\n\t"
        "s_nop 5\n\t"
        "v_pk_mul_f32 v[244:245], v[244:245], v[240:241]\n\t"
        "v_pk_mul_f32 v[246:247], v[246:247], v[242:243]\n\t"
        "v_cvt_pk_bf16_f32 v132, v244, v245\n\t"
        "v_cvt_pk_bf16_f32 v133, v246, v247\n\t"
        "s_waitcnt lgkmcnt(12)\n\t"
        "v_mfma_f32_32x32x16_bf16 v[208:223], v[104:107], v[160:163], v[208:223]\n\t"
        "v_mfma_f32_16x16x32_bf16 v[224:227], v[132:135], v[32:35], 0\n\t"
        "v_mfma_f32_16x16x32_bf16 v[228:231], v[132:135], v[36:39], 0\n\t"
        "v_mfma_f32_16x16x32_bf16 v[232:235], v[132:135], v[40:43], 0\n\t"
        "v_mfma_f32_16x16x32_bf16 v[236:239], v[132:135], v[44:47], 0\n\t"
        "s_waitcnt lgkmcnt(11)\n\t"
        "v_mfma_f32_32x32x16_bf16 v[208:223], v[120:123], v[164:167], v[208:223]\n\t"
        "s_waitcnt lgkmcnt(10)\n\t"
        "v_mfma_f32_32x32x16_bf16 v[208:223], v[108:111], v[168:171], v[208:223]\n\t"
        "s_waitcnt lgkmcnt(9)\n\t"
        "v_mfma_f32_32x32x16_bf16 v[208:223], v[124:127], v[172:175], v[208:223]\n\t"
        "v_cndmask_b32 v8, v224, v228, s[46:47]\n\t"
        "s_nop 0\n\t"
        "v_cndmask_b32 v140, v232, v236, s[46:47]\n\t"
        "s_nop 1\n\t"
        "v_permlane32_swap_b32 v8, v140\n\t"
        "v_add_f32 v22, v22, v8\n\t"
        "v_add_f32 v22, v22, v140\n\t"
        "v_cvt_pk_bf16_f32 v8, v22, v22\n\t"
        "global_store_short v2, v8, s[14:15]\n\t"
        "s_nop 1\n\t"
        "v_cndmask_b32 v208, v208, v216, s[76:77]\n\t"
        "v_cndmask_b32 v209, v209, v217, s[76:77]\n\t"
        "v_cndmask_b32 v210, v210, v218, s[76:77]\n\t"
        "v_cndmask_b32 v211, v211, v219, s[76:77]\n\t"
        "v_cndmask_b32 v212, v212, v220, s[76:77]\n\t"
        "v_cndmask_b32 v213, v213, v221, s[76:77]\n\t"
        "v_cndmask_b32 v214, v214, v222, s[76:77]\n\t"
        "v_cndmask_b32 v215, v215, v223, s[76:77]\n\t"
        "v_exp_f32 v208, v208\n\t"
        "v_exp_f32 v209, v209\n\t"
        "v_exp_f32 v210, v210\n\t"
        "v_exp_f32 v211, v211\n\t"
        "v_exp_f32 v212, v212\n\t"
        "v_exp_f32 v213, v213\n\t"
        "v_exp_f32 v214, v214\n\t"
        "v_exp_f32 v215, v215\n\t"
        "v_pk_mul_f32 v[252:253], v[24:25], 0.5 op_sel_hi:[1,0]\n\t"
        "v_pk_mul_f32 v[254:255], v[26:27], 0.5 op_sel_hi:[1,0]\n\t"
        "v_pk_add_f32 v[208:209], v[208:209], 1.0 op_sel_hi:[1,0]\n\t"
        "v_pk_add_f32 v[210:211], v[210:211], 1.0 op_sel_hi:[1,0]\n\t"
        "v_pk_add_f32 v[212:213], v[212:213], 1.0 op_sel_hi:[1,0]\n\t"
        "v_pk_add_f32 v[214:215], v[214:215], 1.0 op_sel_hi:[1,0]\n\t"
        "v_rcp_f32 v208, v208\n\t"
        "v_rcp_f32 v209, v209\n\t"
        "v_rcp_f32 v210, v210\n\t"
        "v_rcp_f32 v211, v211\n\t"
        "v_rcp_f32 v212, v212\n\t"
        "v_rcp_f32 v213, v213\n\t"
        "v_rcp_f32 v214, v214\n\t"
        "v_rcp_f32 v215, v215\n\t"
        "v_cvt_pk_bf16_f32 v208, v208, v209\n\t"
        "v_cvt_pk_bf16_f32 v209, v210, v211\n\t"
        "v_cvt_pk_bf16_f32 v210, v212, v213\n\t"
        "v_cvt_pk_bf16_f32 v211, v214, v215\n\t"
        "s_nop 1\n\t"
        "v_mfma_f32_16x16x32_bf16 v[252:255], v[208:211], v[184:187], v[252:255]\n\t"
        "s_waitcnt lgkmcnt(8)\n\t"
        "s_nop 6\n\t"
        "v_pk_mul_f32 v[252:253], v[252:253], v[248:249]\n\t"
        "v_pk_mul_f32 v[254:255], v[254:255], v[250:251]\n\t"
        "v_cvt_pk_bf16_f32 v132, v252, v253\n\t"
        "v_cvt_pk_bf16_f32 v133, v254, v255\n\t"
        "s_waitcnt lgkmcnt(6)\n\t"
        "s_nop 0\n\t"
        "v_mfma_f32_16x16x32_bf16 v[208:211], v[132:135], v[48:51], 0\n\t"
        "s_waitcnt lgkmcnt(4)\n\t"
        "v_mfma_f32_16x16x32_bf16 v[212:215], v[132:135], v[52:55], 0\n\t"
        "s_waitcnt lgkmcnt(2)\n\t"
        "v_mfma_f32_16x16x32_bf16 v[216:219], v[132:135], v[56:59], 0\n\t"
        "s_waitcnt lgkmcnt(0)\n\t"
        "v_mfma_f32_16x16x32_bf16 v[220:223], v[132:135], v[60:63], 0\n\t"
        "s_nop 3\n\t"
        "v_cndmask_b32 v8, v208, v212, s[46:47]\n\t"
        "s_nop 2\n\t"
        "v_cndmask_b32 v140, v216, v220, s[46:47]\n\t"
        "s_nop 1\n\t"
        "v_permlane32_swap_b32 v8, v140\n\t"
        "v_add_f32 v23, v23, v8\n\t"
        "v_add_f32 v23, v23, v140\n\t"
        "v_cvt_pk_bf16_f32 v8, v23, v23\n\t"
        "global_store_short v2, v8, s[14:15] offset:128\n\t"
        "s_branch .Ldin_extra\n\t"
        ".Ldin_extra:\n\t"
        "s_mov_b32 s16, s20\n\t"
        ".Ldin_el0:\n\t"
        "s_cmp_ge_u32 s16, s37\n\t"
        "s_cbranch_scc1 .Ldin_ee0\n\t"
        "s_nop 3\n\t"
        "s_lshl_b32 s17, s16, 7\n\t"
        "v_mov_b32 v9, 44032\n\t"
        "s_add_u32 s17, s17, s31\n\t"
        "v_and_b32 v8, 7, v1\n\t"
        "v_lshrrev_b32 v140, 1, v1\n\t"
        "v_and_b32 v140, 16, v140\n\t"
        "v_lshl_or_b32 v137, v8, 7, v140\n\t"
        "v_add_u32 v137, s17, v137\n\t"
        "v_and_b32 v139, 0x60, v29\n\t"
        "v_or_b32 v140, v139, v140\n\t"
        "v_bfe_u32 v139, v1, 2, 3\n\t"
        "v_lshlrev_b32 v136, 3, v1\n\t"
        "v_and_b32 v138, 24, v136\n\t"
        "v_lshl_or_b32 v138, v139, 7, v138\n\t"
        "v_add_u32 v138, s17, v138\n\t"
        "s_sub_u32 s18, 50, s16\n\t"
        "v_cmp_gt_u32 s[76:77], s18, v8\n\t"
        "s_nop 1\n\t"
        "v_cndmask_b32 v137, v9, v137, s[76:77]\n\t"
        "v_cmp_gt_u32 s[76:77], s18, v139\n\t"
        "s_nop 1\n\t"
        "v_cndmask_b32 v138, v9, v138, s[76:77]\n\t"
        "v_and_b32 v139, 0x80, v136\n\t"
        "v_add_u32 v139, v139, v11\n\t"
        "v_cndmask_b32 v136, v137, v9, s[46:47]\n\t"
        "v_cndmask_b32 v137, v9, v137, s[46:47]\n\t"
        "s_lshl2_add_u32 s18, s16, s30\n\t"
        "s_add_u32 s18, s18, 256\n\t"
        "v_and_b32 v8, 16, v1\n\t"
        "v_add_u32 v8, s18, v8\n\t"
        "s_mov_b32 s76, 0x00ff00ff\n\t"
        "s_mov_b32 s77, 0xff00ff00\n\t"
        "v_cndmask_b32 v8, v9, v8, s[76:77]\n\t"
        "s_mov_b32 s76, 0xff00ff00\n\t"
        "ds_read_b128 v[224:227], v139 offset:44288\n\t"
        "ds_read_b128 v[228:231], v139 offset:44304\n\t"
        "ds_read_b128 v[232:235], v139 offset:44352\n\t"
        "ds_read_b128 v[236:239], v139 offset:44368\n\t"
        "ds_read_b128 v[144:147], v136\n\t"
        "ds_read_b128 v[148:151], v137\n\t"
        "ds_read_b128 v[152:155], v136 offset:32\n\t"
        "ds_read_b128 v[156:159], v137 offset:32\n\t"
        "ds_read_b128 v[160:163], v136 offset:64\n\t"
        "ds_read_b128 v[164:167], v137 offset:64\n\t"
        "ds_read_b128 v[168:171], v136 offset:96\n\t"
        "ds_read_b128 v[172:175], v137 offset:96\n\t"
        "ds_read_b128 v[184:187], v140 offset:43264\n\t"
        "ds_read_b128 v[240:243], v8\n\t"
        "ds_read_b64_tr_b16 v[32:33], v138\n\t"
        "ds_read_b64_tr_b16 v[34:35], v138\n\t"
        "ds_read_b64_tr_b16 v[36:37], v138 offset:32\n\t"
        "ds_read_b64_tr_b16 v[38:39], v138 offset:32\n\t"
        "ds_read_b64_tr_b16 v[40:41], v138 offset:64\n\t"
        "ds_read_b64_tr_b16 v[42:43], v138 offset:64\n\t"
        "ds_read_b64_tr_b16 v[44:45], v138 offset:96\n\t"
        "ds_read_b64_tr_b16 v[46:47], v138 offset:96\n\t"
        "s_waitcnt lgkmcnt(15)\n\t"
        "v_mfma_f32_32x32x16_bf16 v[224:239], v[64:67], v[144:147], v[224:239]\n\t"
        "v_mfma_f32_32x32x16_bf16 v[224:239], v[80:83], v[148:151], v[224:239]\n\t"
        "v_mfma_f32_32x32x16_bf16 v[224:239], v[68:71], v[152:155], v[224:239]\n\t"
        "s_waitcnt lgkmcnt(14)\n\t"
        "v_mfma_f32_32x32x16_bf16 v[224:239], v[84:87], v[156:159], v[224:239]\n\t"
        "s_waitcnt lgkmcnt(13)\n\t"
        "v_mfma_f32_32x32x16_bf16 v[224:239], v[72:75], v[160:163], v[224:239]\n\t"
        "s_waitcnt lgkmcnt(12)\n\t"
        "v_mfma_f32_32x32x16_bf16 v[224:239], v[88:91], v[164:167], v[224:239]\n\t"
        "s_waitcnt lgkmcnt(11)\n\t"
        "v_mfma_f32_32x32x16_bf16 v[224:239], v[76:79], v[168:171], v[224:239]\n\t"
        "s_waitcnt lgkmcnt(10)\n\t"
        "v_mfma_f32_32x32x16_bf16 v[224:239], v[92:95], v[172:175], v[224:239]\n\t"
        "s_nop 11\n\t"
        "v_cndmask_b32 v224, v224, v232, s[76:77]\n\t"
        "v_cndmask_b32 v225, v225, v233, s[76:77]\n\t"
        "v_cndmask_b32 v226, v226, v234, s[76:77]\n\t"
        "v_cndmask_b32 v227, v227, v235, s[76:77]\n\t"
        "v_cndmask_b32 v228, v228, v236, s[76:77]\n\t"
        "v_cndmask_b32 v229, v229, v237, s[76:77]\n\t"
        "v_cndmask_b32 v230, v230, v238, s[76:77]\n\t"
        "v_cndmask_b32 v231, v231, v239, s[76:77]\n\t"
        "v_exp_f32 v224, v224\n\t"
        "v_exp_f32 v225, v225\n\t"
        "v_exp_f32 v226, v226\n\t"
        "v_exp_f32 v227, v227\n\t"
        "v_exp_f32 v228, v228\n\t"
        "v_exp_f32 v229, v229\n\t"
        "v_exp_f32 v230, v230\n\t"
        "v_exp_f32 v231, v231\n\t"
        "v_pk_mul_f32 v[244:245], v[24:25], 0.5 op_sel_hi:[1,0]\n\t"
        "v_pk_mul_f32 v[246:247], v[26:27], 0.5 op_sel_hi:[1,0]\n\t"
        "v_pk_add_f32 v[224:225], v[224:225], 1.0 op_sel_hi:[1,0]\n\t"
        "v_pk_add_f32 v[226:227], v[226:227], 1.0 op_sel_hi:[1,0]\n\t"
        "v_pk_add_f32 v[228:229], v[228:229], 1.0 op_sel_hi:[1,0]\n\t"
        "v_pk_add_f32 v[230:231], v[230:231], 1.0 op_sel_hi:[1,0]\n\t"
        "v_rcp_f32 v224, v224\n\t"
        "v_rcp_f32 v225, v225\n\t"
        "v_rcp_f32 v226, v226\n\t"
        "v_rcp_f32 v227, v227\n\t"
        "v_rcp_f32 v228, v228\n\t"
        "v_rcp_f32 v229, v229\n\t"
        "v_rcp_f32 v230, v230\n\t"
        "v_rcp_f32 v231, v231\n\t"
        "v_cvt_pk_bf16_f32 v224, v224, v225\n\t"
        "v_cvt_pk_bf16_f32 v225, v226, v227\n\t"
        "v_cvt_pk_bf16_f32 v226, v228, v229\n\t"
        "v_cvt_pk_bf16_f32 v227, v230, v231\n\t"
        "s_waitcnt lgkmcnt(9)\n\t"
        "s_nop 0\n\t"
        "v_mfma_f32_16x16x32_bf16 v[244:247], v[224:227], v[184:187], v[244:247]\n\t"
        "s_waitcnt lgkmcnt(8)\n\t"
        "s_nop 6\n\t"
        "v_pk_mul_f32 v[244:245], v[244:245], v[240:241]\n\t"
        "v_pk_mul_f32 v[246:247], v[246:247], v[242:243]\n\t"
        "v_cvt_pk_bf16_f32 v132, v244, v245\n\t"
        "v_cvt_pk_bf16_f32 v133, v246, v247\n\t"
        "s_waitcnt lgkmcnt(6)\n\t"
        "s_nop 0\n\t"
        "v_mfma_f32_16x16x32_bf16 v[224:227], v[132:135], v[32:35], 0\n\t"
        "s_waitcnt lgkmcnt(4)\n\t"
        "v_mfma_f32_16x16x32_bf16 v[228:231], v[132:135], v[36:39], 0\n\t"
        "s_waitcnt lgkmcnt(2)\n\t"
        "v_mfma_f32_16x16x32_bf16 v[232:235], v[132:135], v[40:43], 0\n\t"
        "s_waitcnt lgkmcnt(0)\n\t"
        "v_mfma_f32_16x16x32_bf16 v[236:239], v[132:135], v[44:47], 0\n\t"
        "s_nop 3\n\t"
        "v_cndmask_b32 v8, v224, v228, s[46:47]\n\t"
        "s_nop 2\n\t"
        "v_cndmask_b32 v140, v232, v236, s[46:47]\n\t"
        "s_nop 1\n\t"
        "v_permlane32_swap_b32 v8, v140\n\t"
        "v_add_f32 v22, v22, v8\n\t"
        "v_add_f32 v22, v22, v140\n\t"
        "v_cvt_pk_bf16_f32 v8, v22, v22\n\t"
        "global_store_short v2, v8, s[14:15]\n\t"
        "s_add_u32 s16, s16, 8\n\t"
        "s_branch .Ldin_el0\n\t"
        ".Ldin_ee0:\n\t"
        "s_mov_b32 s16, s21\n\t"
        ".Ldin_el1:\n\t"
        "s_cmp_ge_u32 s16, s38\n\t"
        "s_cbranch_scc1 .Ldin_ee1\n\t"
        "s_nop 3\n\t"
        "s_lshl_b32 s17, s16, 7\n\t"
        "v_mov_b32 v9, 44032\n\t"
        "s_add_u32 s17, s17, s3\n\t"
        "v_and_b32 v8, 7, v1\n\t"
        "v_lshrrev_b32 v140, 1, v1\n\t"
        "v_and_b32 v140, 16, v140\n\t"
        "v_lshl_or_b32 v137, v8, 7, v140\n\t"
        "v_add_u32 v137, s17, v137\n\t"
        "v_and_b32 v139, 0x60, v29\n\t"
        "v_or_b32 v140, v139, v140\n\t"
        "v_bfe_u32 v139, v1, 2, 3\n\t"
        "v_lshlrev_b32 v136, 3, v1\n\t"
        "v_and_b32 v138, 24, v136\n\t"
        "v_lshl_or_b32 v138, v139, 7, v138\n\t"
        "v_add_u32 v138, s17, v138\n\t"
        "s_sub_u32 s18, 50, s16\n\t"
        "v_cmp_gt_u32 s[76:77], s18, v8\n\t"
        "s_nop 1\n\t"
        "v_cndmask_b32 v137, v9, v137, s[76:77]\n\t"
        "v_cmp_gt_u32 s[76:77], s18, v139\n\t"
        "s_nop 1\n\t"
        "v_cndmask_b32 v138, v9, v138, s[76:77]\n\t"
        "v_and_b32 v139, 0x80, v136\n\t"
        "v_add_u32 v139, v139, v11\n\t"
        "v_cndmask_b32 v136, v137, v9, s[46:47]\n\t"
        "v_cndmask_b32 v137, v9, v137, s[46:47]\n\t"
        "s_lshl2_add_u32 s18, s16, s30\n\t"
        "s_add_u32 s18, s18, 768\n\t"
        "v_and_b32 v8, 16, v1\n\t"
        "v_add_u32 v8, s18, v8\n\t"
        "s_mov_b32 s76, 0x00ff00ff\n\t"
        "s_mov_b32 s77, 0xff00ff00\n\t"
        "v_cndmask_b32 v8, v9, v8, s[76:77]\n\t"
        "s_mov_b32 s76, 0xff00ff00\n\t"
        "ds_read_b128 v[224:227], v139 offset:44560\n\t"
        "ds_read_b128 v[228:231], v139 offset:44576\n\t"
        "ds_read_b128 v[232:235], v139 offset:44624\n\t"
        "ds_read_b128 v[236:239], v139 offset:44640\n\t"
        "ds_read_b128 v[144:147], v136\n\t"
        "ds_read_b128 v[148:151], v137\n\t"
        "ds_read_b128 v[152:155], v136 offset:32\n\t"
        "ds_read_b128 v[156:159], v137 offset:32\n\t"
        "ds_read_b128 v[160:163], v136 offset:64\n\t"
        "ds_read_b128 v[164:167], v137 offset:64\n\t"
        "ds_read_b128 v[168:171], v136 offset:96\n\t"
        "ds_read_b128 v[172:175], v137 offset:96\n\t"
        "ds_read_b128 v[184:187], v140 offset:43264\n\t"
        "ds_read_b128 v[240:243], v8\n\t"
        "ds_read_b64_tr_b16 v[32:33], v138\n\t"
        "ds_read_b64_tr_b16 v[34:35], v138\n\t"
        "ds_read_b64_tr_b16 v[36:37], v138 offset:32\n\t"
        "ds_read_b64_tr_b16 v[38:39], v138 offset:32\n\t"
        "ds_read_b64_tr_b16 v[40:41], v138 offset:64\n\t"
        "ds_read_b64_tr_b16 v[42:43], v138 offset:64\n\t"
        "ds_read_b64_tr_b16 v[44:45], v138 offset:96\n\t"
        "ds_read_b64_tr_b16 v[46:47], v138 offset:96\n\t"
        "s_waitcnt lgkmcnt(15)\n\t"
        "v_mfma_f32_32x32x16_bf16 v[224:239], v[96:99], v[144:147], v[224:239]\n\t"
        "v_mfma_f32_32x32x16_bf16 v[224:239], v[112:115], v[148:151], v[224:239]\n\t"
        "v_mfma_f32_32x32x16_bf16 v[224:239], v[100:103], v[152:155], v[224:239]\n\t"
        "s_waitcnt lgkmcnt(14)\n\t"
        "v_mfma_f32_32x32x16_bf16 v[224:239], v[116:119], v[156:159], v[224:239]\n\t"
        "s_waitcnt lgkmcnt(13)\n\t"
        "v_mfma_f32_32x32x16_bf16 v[224:239], v[104:107], v[160:163], v[224:239]\n\t"
        "s_waitcnt lgkmcnt(12)\n\t"
        "v_mfma_f32_32x32x16_bf16 v[224:239], v[120:123], v[164:167], v[224:239]\n\t"
        "s_waitcnt lgkmcnt(11)\n\t"
        "v_mfma_f32_32x32x16_bf16 v[224:239], v[108:111], v[168:171], v[224:239]\n\t"
        "s_waitcnt lgkmcnt(10)\n\t"
        "v_mfma_f32_32x32x16_bf16 v[224:239], v[124:127], v[172:175], v[224:239]\n\t"
        "s_nop 11\n\t"
        "v_cndmask_b32 v224, v224, v232, s[76:77]\n\t"
        "v_cndmask_b32 v225, v225, v233, s[76:77]\n\t"
        "v_cndmask_b32 v226, v226, v234, s[76:77]\n\t"
        "v_cndmask_b32 v227, v227, v235, s[76:77]\n\t"
        "v_cndmask_b32 v228, v228, v236, s[76:77]\n\t"
        "v_cndmask_b32 v229, v229, v237, s[76:77]\n\t"
        "v_cndmask_b32 v230, v230, v238, s[76:77]\n\t"
        "v_cndmask_b32 v231, v231, v239, s[76:77]\n\t"
        "v_exp_f32 v224, v224\n\t"
        "v_exp_f32 v225, v225\n\t"
        "v_exp_f32 v226, v226\n\t"
        "v_exp_f32 v227, v227\n\t"
        "v_exp_f32 v228, v228\n\t"
        "v_exp_f32 v229, v229\n\t"
        "v_exp_f32 v230, v230\n\t"
        "v_exp_f32 v231, v231\n\t"
        "v_pk_mul_f32 v[244:245], v[24:25], 0.5 op_sel_hi:[1,0]\n\t"
        "v_pk_mul_f32 v[246:247], v[26:27], 0.5 op_sel_hi:[1,0]\n\t"
        "v_pk_add_f32 v[224:225], v[224:225], 1.0 op_sel_hi:[1,0]\n\t"
        "v_pk_add_f32 v[226:227], v[226:227], 1.0 op_sel_hi:[1,0]\n\t"
        "v_pk_add_f32 v[228:229], v[228:229], 1.0 op_sel_hi:[1,0]\n\t"
        "v_pk_add_f32 v[230:231], v[230:231], 1.0 op_sel_hi:[1,0]\n\t"
        "v_rcp_f32 v224, v224\n\t"
        "v_rcp_f32 v225, v225\n\t"
        "v_rcp_f32 v226, v226\n\t"
        "v_rcp_f32 v227, v227\n\t"
        "v_rcp_f32 v228, v228\n\t"
        "v_rcp_f32 v229, v229\n\t"
        "v_rcp_f32 v230, v230\n\t"
        "v_rcp_f32 v231, v231\n\t"
        "v_cvt_pk_bf16_f32 v224, v224, v225\n\t"
        "v_cvt_pk_bf16_f32 v225, v226, v227\n\t"
        "v_cvt_pk_bf16_f32 v226, v228, v229\n\t"
        "v_cvt_pk_bf16_f32 v227, v230, v231\n\t"
        "s_waitcnt lgkmcnt(9)\n\t"
        "s_nop 0\n\t"
        "v_mfma_f32_16x16x32_bf16 v[244:247], v[224:227], v[184:187], v[244:247]\n\t"
        "s_waitcnt lgkmcnt(8)\n\t"
        "s_nop 6\n\t"
        "v_pk_mul_f32 v[244:245], v[244:245], v[240:241]\n\t"
        "v_pk_mul_f32 v[246:247], v[246:247], v[242:243]\n\t"
        "v_cvt_pk_bf16_f32 v132, v244, v245\n\t"
        "v_cvt_pk_bf16_f32 v133, v246, v247\n\t"
        "s_waitcnt lgkmcnt(6)\n\t"
        "s_nop 0\n\t"
        "v_mfma_f32_16x16x32_bf16 v[224:227], v[132:135], v[32:35], 0\n\t"
        "s_waitcnt lgkmcnt(4)\n\t"
        "v_mfma_f32_16x16x32_bf16 v[228:231], v[132:135], v[36:39], 0\n\t"
        "s_waitcnt lgkmcnt(2)\n\t"
        "v_mfma_f32_16x16x32_bf16 v[232:235], v[132:135], v[40:43], 0\n\t"
        "s_waitcnt lgkmcnt(0)\n\t"
        "v_mfma_f32_16x16x32_bf16 v[236:239], v[132:135], v[44:47], 0\n\t"
        "s_nop 3\n\t"
        "v_cndmask_b32 v8, v224, v228, s[46:47]\n\t"
        "s_nop 2\n\t"
        "v_cndmask_b32 v140, v232, v236, s[46:47]\n\t"
        "s_nop 1\n\t"
        "v_permlane32_swap_b32 v8, v140\n\t"
        "v_add_f32 v23, v23, v8\n\t"
        "v_add_f32 v23, v23, v140\n\t"
        "v_cvt_pk_bf16_f32 v8, v23, v23\n\t"
        "global_store_short v2, v8, s[14:15] offset:128\n\t"
        "s_add_u32 s16, s16, 8\n\t"
        "s_branch .Ldin_el1\n\t"
        ".Ldin_ee1:\n\t"
        ".Ldin_last:\n\t"
        "s_branch .Ldin_end\n\t"
        :: "{s[0:1]}"(__builtin_amdgcn_kernarg_segment_ptr()), "{s2}"(blockIdx.x), "{v0}"(threadIdx.x)
        : "v1", "v2", "v3", "v4", "v5", "v6", "v7", "v8", "v9", "v10", "v11", "v12", "v13", "v14", "v15", "v16", "v17", "v18", "v19", "v20", "v21", "v22", "v23", "v24", "v25", "v26", "v27", "v28", "v29", "v30", "v31", "v32", "v33", "v34", "v35", "v36", "v37", "v38", "v39", "v40", "v41", "v42", "v43", "v44", "v45", "v46", "v47", "v48", "v49", "v50", "v51", "v52", "v53", "v54", "v55", "v56", "v57", "v58", "v59", "v60", "v61", "v62", "v63", "v64", "v65", "v66", "v67", "v68", "v69", "v70", "v71", "v72", "v73", "v74", "v75", "v76", "v77", "v78", "v79", "v80", "v81", "v82", "v83", "v84", "v85", "v86", "v87", "v88", "v89", "v90", "v91", "v92", "v93", "v94", "v95", "v96", "v97", "v98", "v99", "v100", "v101", "v102", "v103", "v104", "v105", "v106", "v107", "v108", "v109", "v110", "v111", "v112", "v113", "v114", "v115", "v116", "v117", "v118", "v119", "v120", "v121", "v122", "v123", "v124", "v125", "v126", "v127", "v128", "v129", "v130", "v131", "v132", "v133", "v134", "v135", "v136", "v137", "v138", "v139", "v140", "v141", "v142", "v143", "v144", "v145", "v146", "v147", "v148", "v149", "v150", "v151", "v152", "v153", "v154", "v155", "v156", "v157", "v158", "v159", "v160", "v161", "v162", "v163", "v164", "v165", "v166", "v167", "v168", "v169", "v170", "v171", "v172", "v173", "v174", "v175", "v176", "v177", "v178", "v179", "v180", "v181", "v182", "v183", "v184", "v185", "v186", "v187", "v188", "v189", "v190", "v191", "v192", "v193", "v194", "v195", "v196", "v197", "v198", "v199", "v200", "v201", "v202", "v203", "v204", "v205", "v206", "v207", "v208", "v209", "v210", "v211", "v212", "v213", "v214", "v215", "v216", "v217", "v218", "v219", "v220", "v221", "v222", "v223", "v224", "v225", "v226", "v227", "v228", "v229", "v230", "v231", "v232", "v233", "v234", "v235", "v236", "v237", "v238", "v239", "v240", "v241", "v242", "v243", "v244", "v245", "v246", "v247", "v248", "v249", "v250", "v251", "v252", "v253", "v254", "v255", "s3", "s4", "s5", "s6", "s7", "s8", "s9", "s10", "s11", "s12", "s13", "s14", "s15", "s16", "s17", "s18", "s19", "s20", "s21", "s22", "s23", "s24", "s25", "s26", "s27", "s28", "s29", "s30", "s31", "s37", "s38", "s39", "s40", "s41", "s42", "s43", "s44", "s45", "s46", "s47", "s48", "s49", "s50", "s51", "s52", "s53", "s54", "s55", "s56", "s57", "s58", "s59", "s60", "s61", "s62", "s63", "s64", "s65", "s66", "s67", "s68", "s69", "s70", "s71", "s72", "s73", "s74", "s75", "s76", "s77", "s78", "s79", "vcc", "scc", "memory");
}

extern "C" void kernel_launch(void* const* d_in, const int* in_sizes, int n_in,
                              void* d_out, int out_size, void* d_ws, size_t ws_size,
                              hipStream_t stream) {
    const int B = in_sizes[0] / 64;
    din_kernel<<<B / NB, NW * 64, 0, stream>>>(d_in[3], d_in[1], d_in[6], d_out, d_in[0], d_in[2], d_in[4], d_in[5]);
}
```
